```python
import torch

def _softmax(x, axis=-1):
    return torch.softmax(x, dim=axis)

def _gelu(x):
    return torch.nn.functional.gelu(x, approximate='tanh')

def _silu(x):
    return torch.nn.functional.silu(x)

def _sigmoid(x):
    return torch.sigmoid(x)

def _layernorm(x, axis=-1, eps=1e-05):
    m = x.mean(dim=axis, keepdim=True)
    v = x.var(dim=axis, keepdim=True, unbiased=False)
    return (x - m) / torch.sqrt(v + eps)

def _scatter_add(tgt, idx, src, axis=0):
    return tgt.clone().index_add_(axis, idx, src)

def _segment_sum(src, seg, n):
    out = torch.zeros((n,) + src.shape[1:], dtype=src.dtype)
    return out.index_add_(0, seg, src)

def _pad(x, pw):
    flat = tuple((v for pair in reversed(pw) for v in pair))
    return torch.nn.functional.pad(x, flat)
kernel_name = 'recurrent_scan__mha_block_cached__mixer_gmlp__prompt'

def setup_inputs():

    _w = lambda a: (lambda t: t.to(torch.bfloat16) if t.is_floating_point() else t)(torch.as_tensor(a))

    def _A_setup():
        B, Sq, D = (1, 2048, 640)
        H, Sk, Dh = (32, 2048, 128)
        return {'x': _w((lambda *s: torch.randn(*s, dtype=torch.bfloat16) * 0.05)(B, Sq, D)), 'k_cache': _w((lambda *s: torch.randn(*s, dtype=torch.bfloat16) * 0.8)(B, H, Sk, Dh)), 'v_cache': _w((lambda *s: torch.randn(*s, dtype=torch.bfloat16) * 0.2)(B, H, Sk, Dh)), 'wq': _w((lambda *s: torch.randn(*s, dtype=torch.bfloat16) * 0.8)(D, H * Dh) * D ** (-0.5)), 'wo': _w((lambda *s: torch.randn(*s, dtype=torch.bfloat16) * 0.2)(H * Dh, D) * (H * Dh) ** (-0.5))}
    _A_in = _A_setup()

    def _B_setup():
        B, S, D, F = (1, 2048, 640, 1728)
        Fh = F // 2
        return {'x': _w((lambda *s: torch.randn(*s, dtype=torch.bfloat16) * 0.05)(B, S, D)), 'w_in': _w((lambda *s: torch.randn(*s, dtype=torch.bfloat16) * 0.4)(D, 2 * Fh)), 'w_sp': _w(0.01 * (lambda *s: torch.randn(*s, dtype=torch.bfloat16) * 0.4)(S, S)), 'b_sp': _w(1.0 + 0.01 * (lambda *s: torch.randn(*s, dtype=torch.bfloat16) * 0.1)(S)), 'w_out': _w((lambda *s: torch.randn(*s, dtype=torch.bfloat16) * 0.1)(Fh, D))}
    _B_in = _B_setup()
    return {'A_x': _A_in['x'], 'A_k_cache': _A_in['k_cache'], 'A_v_cache': _A_in['v_cache'], 'A_wq': _A_in['wq'], 'A_wo': _A_in['wo'], 'B_x': _B_in['x'], 'B_w_in': _B_in['w_in'], 'B_w_sp': _B_in['w_sp'], 'B_b_sp': _B_in['b_sp'], 'B_w_out': _B_in['w_out']}

def reference(A_x, A_k_cache, A_v_cache, A_wq, A_wo, B_x, B_w_in, B_w_sp, B_b_sp, B_w_out):

    def _A_ref(x, k_cache, v_cache, wq, wo):
        B, Sq, D = x.shape
        H, Dh = (32, 128)
        q = (lambda x, *d: x.permute(*d))((x @ wq).reshape(B, Sq, H, Dh), 0, 2, 1, 3)
        scores = torch.einsum('bhqd,bhkd->bhqk', q, k_cache) * Dh ** (-0.5)
        p = _softmax(scores, axis=-1)
        o = torch.einsum('bhqk,bhkd->bhqd', p, v_cache)
        return (lambda x, *d: x.permute(*d))(o, 0, 2, 1, 3).reshape(B, Sq, H * Dh) @ wo

    def _B_ref(x, w_in, w_sp, b_sp, w_out):
        B, S, D = x.shape
        F = w_in.shape[-1]
        Fh = F // 2
        z = _gelu(torch.einsum('bsd,df->bsf', x, w_in))
        z1 = z[..., :Fh].clone()
        z2 = _layernorm(z[..., Fh:], axis=-1)
        g = torch.einsum('ts,bsf->btf', w_sp, z2) + b_sp[None, :, None]
        return torch.einsum('bsf,fd->bsd', z1 * g, w_out)
    x = A_x
    xn = _layernorm(x, axis=-1)
    s = _A_ref(xn, A_k_cache, A_v_cache, A_wq, A_wo)
    g = _silu(_B_ref(xn, B_w_in, B_w_sp, B_b_sp, B_w_out))
    return x + s * g
```

```cpp
#include <hip/hip_runtime.h>
#include <hip/hip_bf16.h>
#include <utility>

constexpr int SEQ = 2048, DM = 640, NH = 32, DH = 128, FH = 864, FF = 1728, HD = NH * DH;
constexpr float THR = 8.0f;
constexpr float QSCALE = 0.12751743082f;
constexpr int NT = SEQ / 64;
constexpr unsigned KRS = 256u, KTILEB = 64u * KRS;
constexpr size_t SHM_ATTN = 163840;
#define STAMP(k, i) do { } while (0)
typedef float    f32x16 __attribute__((ext_vector_type(16)));
typedef float    f32x2  __attribute__((ext_vector_type(2)));
typedef float    f32x4  __attribute__((ext_vector_type(4)));
typedef __bf16   bf16x8 __attribute__((ext_vector_type(8)));
typedef __bf16   bf16x4 __attribute__((ext_vector_type(4)));
typedef __bf16   bf16x2 __attribute__((ext_vector_type(2)));
typedef unsigned u32x4  __attribute__((ext_vector_type(4)));
typedef unsigned u32x2  __attribute__((ext_vector_type(2)));
template <int... Is> using iseq = std::integer_sequence<int, Is...>;
template <int N> using make_iseq = std::make_integer_sequence<int, N>;

#define KRING 0x0000u
#define VRING 0x8000u
#define QSTAGE 0x10000u
#define OSTAGE 0x20000u
#define SRD_WORD3 0x00020000
#define DI __device__ __forceinline__
#define GAP() __builtin_amdgcn_sched_barrier(0)
#define W4_ACLOB_HI "a128","a129","a130","a131","a132","a133","a134","a135","a136","a137","a138","a139","a140","a141","a142","a143","a144","a145","a146","a147","a148","a149","a150","a151","a152","a153","a154","a155","a156","a157","a158","a159","a160","a161","a162","a163","a164","a165","a166","a167","a168","a169","a170","a171","a172","a173","a174","a175","a176","a177","a178","a179","a180","a181","a182","a183","a184","a185","a186","a187","a188","a189","a190","a191","a192","a193","a194","a195","a196","a197","a198","a199","a200","a201","a202","a203","a204","a205","a206","a207","a208","a209","a210","a211","a212","a213","a214","a215","a216","a217","a218","a219","a220","a221","a222","a223","a224","a225","a226","a227","a228","a229","a230","a231","a232","a233","a234","a235","a236","a237","a238","a239","a240","a241","a242","a243","a244","a245","a246","a247","a248","a249","a250","a251","a252","a253","a254","a255"
#define W4_ACLOB "a0","a1","a2","a3","a4","a5","a6","a7","a8","a9","a10","a11","a12","a13","a14","a15","a16","a17","a18","a19","a20","a21","a22","a23","a24","a25","a26","a27","a28","a29","a30","a31","a32","a33","a34","a35","a36","a37","a38","a39","a40","a41","a42","a43","a44","a45","a46","a47","a48","a49","a50","a51","a52","a53","a54","a55","a56","a57","a58","a59","a60","a61","a62","a63","a64","a65","a66","a67","a68","a69","a70","a71","a72","a73","a74","a75","a76","a77","a78","a79","a80","a81","a82","a83","a84","a85","a86","a87","a88","a89","a90","a91","a92","a93","a94","a95","a96","a97","a98","a99","a100","a101","a102","a103","a104","a105","a106","a107","a108","a109","a110","a111","a112","a113","a114","a115","a116","a117","a118","a119","a120","a121","a122","a123","a124","a125","a126","a127"
#define W4_READ_O(DB, QB, o) do { switch (2 * (DB) + (QB)) { \
    case 0: asm volatile("v_accvgpr_read_b32 %0, a0\n\t" "v_accvgpr_read_b32 %1, a1\n\t" "v_accvgpr_read_b32 %2, a2\n\t" "v_accvgpr_read_b32 %3, a3\n\t" "v_accvgpr_read_b32 %4, a4\n\t" "v_accvgpr_read_b32 %5, a5\n\t" "v_accvgpr_read_b32 %6, a6\n\t" "v_accvgpr_read_b32 %7, a7\n\t" "v_accvgpr_read_b32 %8, a8\n\t" "v_accvgpr_read_b32 %9, a9\n\t" "v_accvgpr_read_b32 %10, a10\n\t" "v_accvgpr_read_b32 %11, a11\n\t" "v_accvgpr_read_b32 %12, a12\n\t" "v_accvgpr_read_b32 %13, a13\n\t" "v_accvgpr_read_b32 %14, a14\n\t" "v_accvgpr_read_b32 %15, a15\n\t" : "=v"((o)[0]), "=v"((o)[1]), "=v"((o)[2]), "=v"((o)[3]), "=v"((o)[4]), "=v"((o)[5]), "=v"((o)[6]), "=v"((o)[7]), "=v"((o)[8]), "=v"((o)[9]), "=v"((o)[10]), "=v"((o)[11]), "=v"((o)[12]), "=v"((o)[13]), "=v"((o)[14]), "=v"((o)[15])); break; \
    case 1: asm volatile("v_accvgpr_read_b32 %0, a16\n\t" "v_accvgpr_read_b32 %1, a17\n\t" "v_accvgpr_read_b32 %2, a18\n\t" "v_accvgpr_read_b32 %3, a19\n\t" "v_accvgpr_read_b32 %4, a20\n\t" "v_accvgpr_read_b32 %5, a21\n\t" "v_accvgpr_read_b32 %6, a22\n\t" "v_accvgpr_read_b32 %7, a23\n\t" "v_accvgpr_read_b32 %8, a24\n\t" "v_accvgpr_read_b32 %9, a25\n\t" "v_accvgpr_read_b32 %10, a26\n\t" "v_accvgpr_read_b32 %11, a27\n\t" "v_accvgpr_read_b32 %12, a28\n\t" "v_accvgpr_read_b32 %13, a29\n\t" "v_accvgpr_read_b32 %14, a30\n\t" "v_accvgpr_read_b32 %15, a31\n\t" : "=v"((o)[0]), "=v"((o)[1]), "=v"((o)[2]), "=v"((o)[3]), "=v"((o)[4]), "=v"((o)[5]), "=v"((o)[6]), "=v"((o)[7]), "=v"((o)[8]), "=v"((o)[9]), "=v"((o)[10]), "=v"((o)[11]), "=v"((o)[12]), "=v"((o)[13]), "=v"((o)[14]), "=v"((o)[15])); break; \
    case 2: asm volatile("v_accvgpr_read_b32 %0, a32\n\t" "v_accvgpr_read_b32 %1, a33\n\t" "v_accvgpr_read_b32 %2, a34\n\t" "v_accvgpr_read_b32 %3, a35\n\t" "v_accvgpr_read_b32 %4, a36\n\t" "v_accvgpr_read_b32 %5, a37\n\t" "v_accvgpr_read_b32 %6, a38\n\t" "v_accvgpr_read_b32 %7, a39\n\t" "v_accvgpr_read_b32 %8, a40\n\t" "v_accvgpr_read_b32 %9, a41\n\t" "v_accvgpr_read_b32 %10, a42\n\t" "v_accvgpr_read_b32 %11, a43\n\t" "v_accvgpr_read_b32 %12, a44\n\t" "v_accvgpr_read_b32 %13, a45\n\t" "v_accvgpr_read_b32 %14, a46\n\t" "v_accvgpr_read_b32 %15, a47\n\t" : "=v"((o)[0]), "=v"((o)[1]), "=v"((o)[2]), "=v"((o)[3]), "=v"((o)[4]), "=v"((o)[5]), "=v"((o)[6]), "=v"((o)[7]), "=v"((o)[8]), "=v"((o)[9]), "=v"((o)[10]), "=v"((o)[11]), "=v"((o)[12]), "=v"((o)[13]), "=v"((o)[14]), "=v"((o)[15])); break; \
    case 3: asm volatile("v_accvgpr_read_b32 %0, a48\n\t" "v_accvgpr_read_b32 %1, a49\n\t" "v_accvgpr_read_b32 %2, a50\n\t" "v_accvgpr_read_b32 %3, a51\n\t" "v_accvgpr_read_b32 %4, a52\n\t" "v_accvgpr_read_b32 %5, a53\n\t" "v_accvgpr_read_b32 %6, a54\n\t" "v_accvgpr_read_b32 %7, a55\n\t" "v_accvgpr_read_b32 %8, a56\n\t" "v_accvgpr_read_b32 %9, a57\n\t" "v_accvgpr_read_b32 %10, a58\n\t" "v_accvgpr_read_b32 %11, a59\n\t" "v_accvgpr_read_b32 %12, a60\n\t" "v_accvgpr_read_b32 %13, a61\n\t" "v_accvgpr_read_b32 %14, a62\n\t" "v_accvgpr_read_b32 %15, a63\n\t" : "=v"((o)[0]), "=v"((o)[1]), "=v"((o)[2]), "=v"((o)[3]), "=v"((o)[4]), "=v"((o)[5]), "=v"((o)[6]), "=v"((o)[7]), "=v"((o)[8]), "=v"((o)[9]), "=v"((o)[10]), "=v"((o)[11]), "=v"((o)[12]), "=v"((o)[13]), "=v"((o)[14]), "=v"((o)[15])); break; \
    case 4: asm volatile("v_accvgpr_read_b32 %0, a64\n\t" "v_accvgpr_read_b32 %1, a65\n\t" "v_accvgpr_read_b32 %2, a66\n\t" "v_accvgpr_read_b32 %3, a67\n\t" "v_accvgpr_read_b32 %4, a68\n\t" "v_accvgpr_read_b32 %5, a69\n\t" "v_accvgpr_read_b32 %6, a70\n\t" "v_accvgpr_read_b32 %7, a71\n\t" "v_accvgpr_read_b32 %8, a72\n\t" "v_accvgpr_read_b32 %9, a73\n\t" "v_accvgpr_read_b32 %10, a74\n\t" "v_accvgpr_read_b32 %11, a75\n\t" "v_accvgpr_read_b32 %12, a76\n\t" "v_accvgpr_read_b32 %13, a77\n\t" "v_accvgpr_read_b32 %14, a78\n\t" "v_accvgpr_read_b32 %15, a79\n\t" : "=v"((o)[0]), "=v"((o)[1]), "=v"((o)[2]), "=v"((o)[3]), "=v"((o)[4]), "=v"((o)[5]), "=v"((o)[6]), "=v"((o)[7]), "=v"((o)[8]), "=v"((o)[9]), "=v"((o)[10]), "=v"((o)[11]), "=v"((o)[12]), "=v"((o)[13]), "=v"((o)[14]), "=v"((o)[15])); break; \
    case 5: asm volatile("v_accvgpr_read_b32 %0, a80\n\t" "v_accvgpr_read_b32 %1, a81\n\t" "v_accvgpr_read_b32 %2, a82\n\t" "v_accvgpr_read_b32 %3, a83\n\t" "v_accvgpr_read_b32 %4, a84\n\t" "v_accvgpr_read_b32 %5, a85\n\t" "v_accvgpr_read_b32 %6, a86\n\t" "v_accvgpr_read_b32 %7, a87\n\t" "v_accvgpr_read_b32 %8, a88\n\t" "v_accvgpr_read_b32 %9, a89\n\t" "v_accvgpr_read_b32 %10, a90\n\t" "v_accvgpr_read_b32 %11, a91\n\t" "v_accvgpr_read_b32 %12, a92\n\t" "v_accvgpr_read_b32 %13, a93\n\t" "v_accvgpr_read_b32 %14, a94\n\t" "v_accvgpr_read_b32 %15, a95\n\t" : "=v"((o)[0]), "=v"((o)[1]), "=v"((o)[2]), "=v"((o)[3]), "=v"((o)[4]), "=v"((o)[5]), "=v"((o)[6]), "=v"((o)[7]), "=v"((o)[8]), "=v"((o)[9]), "=v"((o)[10]), "=v"((o)[11]), "=v"((o)[12]), "=v"((o)[13]), "=v"((o)[14]), "=v"((o)[15])); break; \
    case 6: asm volatile("v_accvgpr_read_b32 %0, a96\n\t" "v_accvgpr_read_b32 %1, a97\n\t" "v_accvgpr_read_b32 %2, a98\n\t" "v_accvgpr_read_b32 %3, a99\n\t" "v_accvgpr_read_b32 %4, a100\n\t" "v_accvgpr_read_b32 %5, a101\n\t" "v_accvgpr_read_b32 %6, a102\n\t" "v_accvgpr_read_b32 %7, a103\n\t" "v_accvgpr_read_b32 %8, a104\n\t" "v_accvgpr_read_b32 %9, a105\n\t" "v_accvgpr_read_b32 %10, a106\n\t" "v_accvgpr_read_b32 %11, a107\n\t" "v_accvgpr_read_b32 %12, a108\n\t" "v_accvgpr_read_b32 %13, a109\n\t" "v_accvgpr_read_b32 %14, a110\n\t" "v_accvgpr_read_b32 %15, a111\n\t" : "=v"((o)[0]), "=v"((o)[1]), "=v"((o)[2]), "=v"((o)[3]), "=v"((o)[4]), "=v"((o)[5]), "=v"((o)[6]), "=v"((o)[7]), "=v"((o)[8]), "=v"((o)[9]), "=v"((o)[10]), "=v"((o)[11]), "=v"((o)[12]), "=v"((o)[13]), "=v"((o)[14]), "=v"((o)[15])); break; \
    case 7: asm volatile("v_accvgpr_read_b32 %0, a112\n\t" "v_accvgpr_read_b32 %1, a113\n\t" "v_accvgpr_read_b32 %2, a114\n\t" "v_accvgpr_read_b32 %3, a115\n\t" "v_accvgpr_read_b32 %4, a116\n\t" "v_accvgpr_read_b32 %5, a117\n\t" "v_accvgpr_read_b32 %6, a118\n\t" "v_accvgpr_read_b32 %7, a119\n\t" "v_accvgpr_read_b32 %8, a120\n\t" "v_accvgpr_read_b32 %9, a121\n\t" "v_accvgpr_read_b32 %10, a122\n\t" "v_accvgpr_read_b32 %11, a123\n\t" "v_accvgpr_read_b32 %12, a124\n\t" "v_accvgpr_read_b32 %13, a125\n\t" "v_accvgpr_read_b32 %14, a126\n\t" "v_accvgpr_read_b32 %15, a127\n\t" : "=v"((o)[0]), "=v"((o)[1]), "=v"((o)[2]), "=v"((o)[3]), "=v"((o)[4]), "=v"((o)[5]), "=v"((o)[6]), "=v"((o)[7]), "=v"((o)[8]), "=v"((o)[9]), "=v"((o)[10]), "=v"((o)[11]), "=v"((o)[12]), "=v"((o)[13]), "=v"((o)[14]), "=v"((o)[15])); break; \
    } } while (0)

DI unsigned rfl(unsigned x) { return __builtin_amdgcn_readfirstlane(x); }
DI unsigned cvtpk(float lo, float hi) {
  f32x2 v = {lo, hi}; bf16x2 b = __builtin_convertvector(v, bf16x2); return __builtin_bit_cast(unsigned, b);
}
DI float max3f(float a, float b, float c) { float r; asm("v_max3_f32 %0, %1, %2, %3" : "=v"(r) : "v"(a), "v"(b), "v"(c)); return r; }
DI float max2f(float a, float b) { float r; asm("v_max_f32_e32 %0, %1, %2" : "=v"(r) : "v"(a), "v"(b)); return r; }
DI float fadd_s(float a, float b) { float r; asm("v_add_f32_e32 %0, %1, %2" : "=v"(r) : "v"(a), "v"(b)); return r; }
DI float fmul_s(float a, float b) { float r; asm("v_mul_f32_e32 %0, %1, %2" : "=v"(r) : "v"(a), "v"(b)); return r; }
DI float fsub_s(float a, float b) { float r; asm("v_sub_f32_e32 %0, %1, %2" : "=v"(r) : "v"(a), "v"(b)); return r; }
DI float w4_aone() { unsigned l; asm volatile("v_mbcnt_lo_u32_b32 %0, -1, 0\n\tv_mbcnt_hi_u32_b32 %0, -1, %0" : "=v"(l)); return l < 32u ? 1.f : 0.f; }
DI void negm_bcast(f32x16& negm, float aone, float neg_mhat) {
  asm volatile("s_nop 1\n\tv_mfma_f32_32x32x2_f32 %0, %1, %2, 0" : "+v"(negm) : "v"(aone), "v"(neg_mhat));
}

struct W4C {
  unsigned vkb[4], vvb, vksrc[2], vvsrc, m0k, m0v, sofk, sofv, ldsb;
  __amdgpu_buffer_rsrc_t srdK, srdV;
  __amdgpu_buffer_rsrc_t srdKn, srdVn, srdQn; unsigned qsofn, qstepn, m0q;
  int T; float THR;
  f32x16 S[2][2][2];
  f32x16 NEGM[2];
  bf16x8 Vf[4][4];
  bf16x8 Pk[2][4];
  bf16x4 vlo[4][4], vup[4][4];
  float mhat[2], lsum[2], fct[2]; bool pend;
  float rs0[2], rs1[2], pmx0[2], pmx1[2], rmax[2], delta[2];
  unsigned pm0[8], psf[8];
};

constexpr int AO_(int db, int qb) { return 16 * (2 * db + qb); }
constexpr int AQ_(int qb, int d0) { return 128 + 32 * qb + 4 * d0; }
constexpr int AK_(int kb, int d0) { return 192 + 32 * kb + 4 * d0; }

template <int I> DI void tr_read(W4C& c, unsigned vslot) {
  constexpr int ks = I >> 3, db = (I >> 1) & 3;
  if constexpr ((I & 1) == 0) asm volatile("ds_read_b64_tr_b16 %0, %1 offset:%2" : "=v"(c.vlo[db][ks]) : "v"(c.vvb + vslot), "i"(4096 * ks + 512 * db) : "memory");
  else                        asm volatile("ds_read_b64_tr_b16 %0, %1 offset:%2" : "=v"(c.vup[db][ks]) : "v"(c.vvb + vslot), "i"(4096 * ks + 2048 + 512 * db) : "memory");
}
template <int I> DI void k_read(W4C& c, unsigned slot) {
  constexpr int kb = I >> 3, d0 = I & 7;
  asm volatile("ds_read_b128 a[%c1:%c2], %0 offset:%c3" :: "v"(c.vkb[d0 & 3] + slot), "i"(AK_(kb, d0)), "i"(AK_(kb, d0) + 3), "i"(8192 * kb + 128 * (d0 >> 2)) : "memory", W4_ACLOB_HI);
}
template <int I> DI void q_read(W4C& c, unsigned qoff) {
  constexpr int qb = I >> 3, d0 = I & 7;
  asm volatile("ds_read_b128 a[%c1:%c2], %0 offset:%c3" :: "v"(c.vkb[d0 & 3] + qoff), "i"(AQ_(qb, d0)), "i"(AQ_(qb, d0) + 3), "i"(8192 * qb + 128 * (d0 >> 2)) : "memory", W4_ACLOB_HI);
}
DI bf16x8 q_ldread(unsigned base, const int qb, const int d0) {
  bf16x8 r;
  switch (2 * qb + (d0 >> 2)) {
    case 0: asm volatile("ds_read_b128 %0, %1 offset:0"    : "=v"(r) : "v"(base) : "memory"); break;
    case 1: asm volatile("ds_read_b128 %0, %1 offset:128"  : "=v"(r) : "v"(base) : "memory"); break;
    case 2: asm volatile("ds_read_b128 %0, %1 offset:8192" : "=v"(r) : "v"(base) : "memory"); break;
    default: asm volatile("ds_read_b128 %0, %1 offset:8320" : "=v"(r) : "v"(base) : "memory"); break;
  }
  return r;
}
template <int I> DI void q_write1(const u32x4& w) {
  constexpr int aq = AQ_(I >> 3, I & 7);
  asm volatile("v_accvgpr_write_b32 a[%c4], %0\n\tv_accvgpr_write_b32 a[%c5], %1\n\tv_accvgpr_write_b32 a[%c6], %2\n\tv_accvgpr_write_b32 a[%c7], %3"
               :: "v"(w[0]), "v"(w[1]), "v"(w[2]), "v"(w[3]), "i"(aq), "i"(aq + 1), "i"(aq + 2), "i"(aq + 3) : W4_ACLOB_HI);
}
DI void q_write(const int qb, const int d0, const u32x4& w) {
  switch (8 * qb + d0) {
    case 0: q_write1<0>(w); break;   case 1: q_write1<1>(w); break;   case 2: q_write1<2>(w); break;   case 3: q_write1<3>(w); break;
    case 4: q_write1<4>(w); break;   case 5: q_write1<5>(w); break;   case 6: q_write1<6>(w); break;   case 7: q_write1<7>(w); break;
    case 8: q_write1<8>(w); break;   case 9: q_write1<9>(w); break;   case 10: q_write1<10>(w); break; case 11: q_write1<11>(w); break;
    case 12: q_write1<12>(w); break; case 13: q_write1<13>(w); break; case 14: q_write1<14>(w); break; case 15: q_write1<15>(w); break;
  }
}
template <int X, bool FIRST, int M> DI void qk_mfma(W4C& c) {
  constexpr int d0 = M >> 2, kb = (M >> 1) & 1, qb = M & 1, ak = AK_(kb, d0), aq = AQ_(qb, d0);
  if constexpr (d0 == 0) {
    if constexpr (FIRST) asm volatile("v_mfma_f32_32x32x16_bf16 %0, a[%c1:%c2], a[%c3:%c4], 0" : "=&v"(c.S[X][kb][qb]) : "i"(ak), "i"(ak + 3), "i"(aq), "i"(aq + 3) : W4_ACLOB, W4_ACLOB_HI);
    else                 asm volatile("v_mfma_f32_32x32x16_bf16 %0, a[%c2:%c3], a[%c4:%c5], %1" : "=&v"(c.S[X][kb][qb]) : "v"(c.NEGM[qb]), "i"(ak), "i"(ak + 3), "i"(aq), "i"(aq + 3) : W4_ACLOB, W4_ACLOB_HI);
  } else asm volatile("v_mfma_f32_32x32x16_bf16 %0, a[%c1:%c2], a[%c3:%c4], %0" : "+v"(c.S[X][kb][qb]) : "i"(ak), "i"(ak + 3), "i"(aq), "i"(aq + 3) : W4_ACLOB, W4_ACLOB_HI);
}
template <int M, bool ZERO> DI void pv_mfma(W4C& c) {
  constexpr int ks = M >> 3, db = (M >> 1) & 3, qb = M & 1, ao = AO_(db, qb);
  if constexpr (ZERO && ks == 0) asm volatile("v_mfma_f32_32x32x16_bf16 a[%c2:%c3], %0, %1, 0" :: "v"(c.Vf[db][ks]), "v"(c.Pk[qb][ks]), "i"(ao), "i"(ao + 15) : W4_ACLOB);
  else                         asm volatile("v_mfma_f32_32x32x16_bf16 a[%c2:%c3], %0, %1, a[%c2:%c3]" :: "v"(c.Vf[db][ks]), "v"(c.Pk[qb][ks]), "i"(ao), "i"(ao + 15) : W4_ACLOB);
}
template <int X, int QB, int KS, int P> DI void pack1(W4C& c) {
  u32x4 w = __builtin_bit_cast(u32x4, c.Pk[QB][KS]);
  w[P] = cvtpk(c.S[X][KS >> 1][QB][8 * (KS & 1) + 2 * P], c.S[X][KS >> 1][QB][8 * (KS & 1) + 2 * P + 1]);
  c.Pk[QB][KS] = __builtin_bit_cast(bf16x8, w);
}
template <int X, int J> DI void rowsum1(W4C& c) {
  constexpr int qb = J >> 4, ks = (J >> 2) & 3, p = J & 3;
  c.rs0[qb] = fadd_s(c.rs0[qb], c.S[X][ks >> 1][qb][8 * (ks & 1) + 2 * p]); c.rs1[qb] = fadd_s(c.rs1[qb], c.S[X][ks >> 1][qb][8 * (ks & 1) + 2 * p + 1]);
}

template <int X, bool FIRST, int M, bool NEXTK = false, bool NEXTV = false> DI void gapA(W4C& c, unsigned vslot, const int t) {
  constexpr int Xp = X ^ 1;
  qk_mfma<X, FIRST, M>(c);
  if constexpr (M >= 16 && M <= 31) {
    const int tk = NEXTK ? t + 2 - c.T : t + 2, tv = NEXTV ? t + 1 - c.T : t + 1;
    constexpr int q = (M - 16) >> 1;
    if constexpr (((M - 16) & 1) == 0) {
      if constexpr (q < 4) c.pm0[q] = c.m0k + (unsigned)((t + 2) & 1) * 0x4000u + 1024u * q; else c.pm0[q] = c.m0v + (unsigned)((t + 1) & 1) * 0x4000u + 1024u * (q - 4);
      asm volatile("" : "+s"(c.pm0[q]));
    } else {
      if constexpr (q < 4) c.psf[q] = KTILEB * (unsigned)tk + c.sofk + KRS * 4u * q; else c.psf[q] = KTILEB * (unsigned)tv + c.sofv + KRS * 8u * ((q - 4) >> 1) + 128u * ((q - 4) & 1);
      asm volatile("" : "+s"(c.psf[q]));
    }
  }
  if constexpr (M == 31) asm volatile("s_nop 4" : "+s"(c.pm0[0]), "+s"(c.pm0[1]), "+s"(c.pm0[2]), "+s"(c.pm0[3]), "+s"(c.pm0[4]), "+s"(c.pm0[5]), "+s"(c.pm0[6]), "+s"(c.pm0[7]),
                                                  "+s"(c.psf[0]), "+s"(c.psf[1]), "+s"(c.psf[2]), "+s"(c.psf[3]), "+s"(c.psf[4]), "+s"(c.psf[5]), "+s"(c.psf[6]), "+s"(c.psf[7]));
  if constexpr (!FIRST && M == 8) asm volatile("" :: "v"(c.NEGM[0]), "v"(c.NEGM[1]));
  if constexpr (!FIRST) {
    constexpr int base = M + (M >= 4) + (M >= 8) + (M >= 12) + (M >= 16) + (M >= 20) + (M >= 24);
    if constexpr (M <= 25) tr_read<base>(c, vslot);
    if constexpr (M == 3 || M == 7 || M == 11 || M == 15 || M == 19 || M == 23) tr_read<base + 1>(c, vslot);
    if constexpr (M < 16) {
      constexpr int e0 = 2 * M, e1 = 2 * M + 1;
      c.S[Xp][1][e0 >> 4][e0 & 15] = __builtin_amdgcn_exp2f(c.S[Xp][1][e0 >> 4][e0 & 15]);
      c.S[Xp][1][e1 >> 4][e1 & 15] = __builtin_amdgcn_exp2f(c.S[Xp][1][e1 >> 4][e1 & 15]);
      pack1<Xp, (M >> 3), ((M >> 2) & 1), (M & 3)>(c);
    } else {
      constexpr int cc = M - 16;
      pack1<Xp, (cc >> 3), 2 + ((cc >> 2) & 1), (cc & 3)>(c);
      rowsum1<Xp, 16 * (cc >> 3) + 8 + 4 * ((cc >> 2) & 1) + (cc & 3)>(c);
    }
  }
  GAP();
}
template <int X, bool FIRST, bool NEXTK, bool NEXTV, int VW, int... Ms> DI void phaseA_(W4C& c, const int t, iseq<Ms...>) {
  const unsigned vslot = (unsigned)((t - 1) & 1) * 0x4000u;
  (gapA<X, FIRST, Ms, NEXTK, NEXTV>(c, vslot, t), ...);
  if constexpr (!FIRST) {
    #pragma unroll
    for (int qb = 0; qb < 2; ++qb) {
      const float rs = fadd_s(c.rs0[qb], c.rs1[qb]);
      auto pr = __builtin_amdgcn_permlane32_swap(__float_as_uint(rs), __float_as_uint(rs), false, false);
      c.lsum[qb] = fadd_s(c.lsum[qb], fadd_s(__uint_as_float(pr[0]), __uint_as_float(pr[1])));
    }
  }
#define W4_VLO "+v"(c.vlo[0][0]), "+v"(c.vlo[0][1]), "+v"(c.vlo[0][2]), "+v"(c.vlo[0][3]), "+v"(c.vlo[1][0]), "+v"(c.vlo[1][1]), "+v"(c.vlo[1][2]), "+v"(c.vlo[1][3]), \
               "+v"(c.vlo[2][0]), "+v"(c.vlo[2][1]), "+v"(c.vlo[2][2]), "+v"(c.vlo[2][3]), "+v"(c.vlo[3][0]), "+v"(c.vlo[3][1]), "+v"(c.vlo[3][2]), "+v"(c.vlo[3][3])
#define W4_VUP "+v"(c.vup[0][0]), "+v"(c.vup[0][1]), "+v"(c.vup[0][2]), "+v"(c.vup[0][3]), "+v"(c.vup[1][0]), "+v"(c.vup[1][1]), "+v"(c.vup[1][2]), "+v"(c.vup[1][3]), \
               "+v"(c.vup[2][0]), "+v"(c.vup[2][1]), "+v"(c.vup[2][2]), "+v"(c.vup[2][3]), "+v"(c.vup[3][0]), "+v"(c.vup[3][1]), "+v"(c.vup[3][2]), "+v"(c.vup[3][3])
  if constexpr (FIRST) asm volatile("s_waitcnt vmcnt(0) lgkmcnt(0)\n\ts_barrier" ::: "memory");
  else { asm volatile("s_waitcnt vmcnt(%c16) lgkmcnt(0)" : W4_VLO : "i"(VW) : "memory"); asm volatile("s_barrier" : W4_VUP :: "memory"); }
  GAP();
  if constexpr (!FIRST) {
    #pragma unroll
    for (int ks = 0; ks < 4; ++ks)
      #pragma unroll
      for (int db = 0; db < 4; ++db) c.Vf[db][ks] = __builtin_shufflevector(c.vlo[db][ks], c.vup[db][ks], 0, 1, 2, 3, 4, 5, 6, 7);
  }
}
template <int X, bool FIRST, bool NEXTK = false, bool NEXTV = false, int VW = 0> DI void phaseA(W4C& c, const int t) { phaseA_<X, FIRST, NEXTK, NEXTV, VW>(c, t, make_iseq<32>{}); }

template <int X, bool PEEL, bool ZERO1, int DK, int DV, bool KR, int M> DI void gapB(W4C& c, const unsigned kslot_rd) {
  if constexpr (!PEEL) pv_mfma<M, ZERO1>(c);
  if constexpr (M < 4 && DK == 1)             asm volatile("s_mov_b32 m0, %0\n\ts_nop 0\n\tbuffer_load_dwordx4 %1, %2, %3 offen lds" :: "s"(c.pm0[M]), "v"(c.vksrc[M & 1]), "s"(c.srdK),  "s"(c.psf[M]) : "m0", "memory");
  else if constexpr (M < 4 && DK == 2)        asm volatile("s_mov_b32 m0, %0\n\ts_nop 0\n\tbuffer_load_dwordx4 %1, %2, %3 offen lds" :: "s"(c.pm0[M]), "v"(c.vksrc[M & 1]), "s"(c.srdKn), "s"(c.psf[M]) : "m0", "memory");
  else if constexpr (M >= 4 && M < 8 && DV == 1) asm volatile("s_mov_b32 m0, %0\n\ts_nop 0\n\tbuffer_load_dwordx4 %1, %2, %3 offen lds" :: "s"(c.pm0[M]), "v"(c.vvsrc), "s"(c.srdV),  "s"(c.psf[M]) : "m0", "memory");
  else if constexpr (M >= 4 && M < 8 && DV == 2) asm volatile("s_mov_b32 m0, %0\n\ts_nop 0\n\tbuffer_load_dwordx4 %1, %2, %3 offen lds" :: "s"(c.pm0[M]), "v"(c.vvsrc), "s"(c.srdVn), "s"(c.psf[M]) : "m0", "memory");
  if constexpr (M >= 1 && M <= 16 && KR) k_read<M - 1>(c, kslot_rd);
  if constexpr (M >= 8 && M < 16) {
    constexpr int i = M - 8, q = i >> 2, r = 4 * (i & 3);
    if constexpr ((i & 3) == 0) { c.pmx0[q] = max3f(c.S[X][0][q][0], c.S[X][0][q][1], c.S[X][1][q][0]); c.pmx1[q] = max3f(c.S[X][0][q][2], c.S[X][0][q][3], c.S[X][1][q][1]);
                                  c.pmx0[q] = max3f(c.pmx0[q], c.S[X][1][q][2], c.S[X][1][q][3]); }
    else                        { c.pmx0[q] = max3f(c.pmx0[q], c.S[X][0][q][r], c.S[X][0][q][r + 1]); c.pmx1[q] = max3f(c.pmx1[q], c.S[X][0][q][r + 2], c.S[X][0][q][r + 3]);
                                  c.pmx0[q] = max3f(c.pmx0[q], c.S[X][1][q][r], c.S[X][1][q][r + 1]); c.pmx1[q] = max3f(c.pmx1[q], c.S[X][1][q][r + 2], c.S[X][1][q][r + 3]); }
  }
  if constexpr (M == 16 || M == 17) {
    constexpr int q = M - 16; const float mm = max2f(c.pmx0[q], c.pmx1[q]);
    auto pr = __builtin_amdgcn_permlane32_swap(__float_as_uint(mm), __float_as_uint(mm), false, false);
    c.rmax[q] = max2f(__uint_as_float(pr[0]), __uint_as_float(pr[1]));
  }
  if constexpr (M == 18) {
    if constexpr (PEEL) {
      #pragma unroll
      for (int q = 0; q < 2; ++q) { c.mhat[q] = c.rmax[q]; c.lsum[q] = 0.f; c.fct[q] = 1.f;
        #pragma unroll
        for (int kb = 0; kb < 2; ++kb) { _Pragma("unroll") for (int e = 0; e < 16; ++e) c.S[X][kb][q][e] = fsub_s(c.S[X][kb][q][e], c.rmax[q]); }
        negm_bcast(c.NEGM[q], w4_aone(), -c.mhat[q]);
      }
    } else {
      const bool grew = (c.rmax[0] > c.THR) | (c.rmax[1] > c.THR);
      if (__builtin_expect(__any((int)grew), 0)) {
        #pragma unroll
        for (int q = 0; q < 2; ++q) { const float dl = max2f(c.rmax[q], 0.f); c.delta[q] = dl; c.mhat[q] = fadd_s(c.mhat[q], dl); c.fct[q] = __builtin_amdgcn_exp2f(-dl);
          #pragma unroll
          for (int kb = 0; kb < 2; ++kb) { _Pragma("unroll") for (int e = 0; e < 16; ++e) c.S[X][kb][q][e] = fsub_s(c.S[X][kb][q][e], dl); }
          negm_bcast(c.NEGM[q], w4_aone(), -c.mhat[q]);
        }
        c.pend = true;
      }
    }
  }
  if constexpr (M == 18) { c.rs0[0] = c.rs0[1] = c.rs1[0] = c.rs1[1] = 0.f; }
  if constexpr (M >= 19) {
    constexpr int g = M - 19, e_lo = (32 * g) / 13, e_hi = (32 * (g + 1)) / 13;
    {
      constexpr int p_lo = g ? (32 * (g - 1)) / 13 : 0, p_hi = g ? (32 * g) / 13 : 0;
      if constexpr (p_lo + 0 < p_hi) { constexpr int e = p_lo + 0; if constexpr (e & 1) c.rs1[e >> 4] = fadd_s(c.rs1[e >> 4], c.S[X][0][e >> 4][e & 15]); else c.rs0[e >> 4] = fadd_s(c.rs0[e >> 4], c.S[X][0][e >> 4][e & 15]); }
      if constexpr (p_lo + 1 < p_hi) { constexpr int e = p_lo + 1; if constexpr (e & 1) c.rs1[e >> 4] = fadd_s(c.rs1[e >> 4], c.S[X][0][e >> 4][e & 15]); else c.rs0[e >> 4] = fadd_s(c.rs0[e >> 4], c.S[X][0][e >> 4][e & 15]); }
      if constexpr (p_lo + 2 < p_hi) { constexpr int e = p_lo + 2; if constexpr (e & 1) c.rs1[e >> 4] = fadd_s(c.rs1[e >> 4], c.S[X][0][e >> 4][e & 15]); else c.rs0[e >> 4] = fadd_s(c.rs0[e >> 4], c.S[X][0][e >> 4][e & 15]); }
    }
    if constexpr (e_lo + 0 < e_hi) c.S[X][0][(e_lo + 0) >> 4][(e_lo + 0) & 15] = __builtin_amdgcn_exp2f(c.S[X][0][(e_lo + 0) >> 4][(e_lo + 0) & 15]);
    if constexpr (e_lo + 1 < e_hi) c.S[X][0][(e_lo + 1) >> 4][(e_lo + 1) & 15] = __builtin_amdgcn_exp2f(c.S[X][0][(e_lo + 1) >> 4][(e_lo + 1) & 15]);
    if constexpr (e_lo + 2 < e_hi) c.S[X][0][(e_lo + 2) >> 4][(e_lo + 2) & 15] = __builtin_amdgcn_exp2f(c.S[X][0][(e_lo + 2) >> 4][(e_lo + 2) & 15]);
  }
  if constexpr (M == 31) asm volatile("" :: "v"(c.S[X][0][0]), "v"(c.S[X][0][1]), "v"(c.NEGM[0]), "v"(c.NEGM[1]), "v"(c.rs0[0]), "v"(c.rs0[1]), "v"(c.rs1[0]), "v"(c.rs1[1]));
  GAP();
}
template <int X, bool PEEL, bool ZERO1, int DK, int DV, bool KR, int... Ms> DI void phaseB_(W4C& c, const int t, iseq<Ms...>) {
  const unsigned kslot_rd = (unsigned)((t + 1) & 1) * 0x4000u;
  if constexpr (!PEEL) asm volatile("s_nop 1" ::: "memory");
  GAP();
  (gapB<X, PEEL, ZERO1, DK, DV, KR, Ms>(c, kslot_rd), ...);
  {
    #pragma unroll
    for (int e = (32 * 12) / 13; e < 32; ++e) { if (e & 1) c.rs1[e >> 4] = fadd_s(c.rs1[e >> 4], c.S[X][0][e >> 4][e & 15]); else c.rs0[e >> 4] = fadd_s(c.rs0[e >> 4], c.S[X][0][e >> 4][e & 15]); }
  }
  if constexpr (!PEEL) {
    if (__builtin_expect(c.pend, 0)) {
      float t0, t1;
      asm volatile("s_nop 7\n\ts_nop 7\n\t"
        "v_accvgpr_read_b32 %0, a0\n\tv_mul_f32 %0, %2, %0\n\tv_accvgpr_write_b32 a0, %0\n\t"
        "v_accvgpr_read_b32 %1, a1\n\tv_mul_f32 %1, %2, %1\n\tv_accvgpr_write_b32 a1, %1\n\t"
        "v_accvgpr_read_b32 %0, a2\n\tv_mul_f32 %0, %2, %0\n\tv_accvgpr_write_b32 a2, %0\n\t"
        "v_accvgpr_read_b32 %1, a3\n\tv_mul_f32 %1, %2, %1\n\tv_accvgpr_write_b32 a3, %1\n\t"
        "v_accvgpr_read_b32 %0, a4\n\tv_mul_f32 %0, %2, %0\n\tv_accvgpr_write_b32 a4, %0\n\t"
        "v_accvgpr_read_b32 %1, a5\n\tv_mul_f32 %1, %2, %1\n\tv_accvgpr_write_b32 a5, %1\n\t"
        "v_accvgpr_read_b32 %0, a6\n\tv_mul_f32 %0, %2, %0\n\tv_accvgpr_write_b32 a6, %0\n\t"
        "v_accvgpr_read_b32 %1, a7\n\tv_mul_f32 %1, %2, %1\n\tv_accvgpr_write_b32 a7, %1\n\t"
        "v_accvgpr_read_b32 %0, a8\n\tv_mul_f32 %0, %2, %0\n\tv_accvgpr_write_b32 a8, %0\n\t"
        "v_accvgpr_read_b32 %1, a9\n\tv_mul_f32 %1, %2, %1\n\tv_accvgpr_write_b32 a9, %1\n\t"
        "v_accvgpr_read_b32 %0, a10\n\tv_mul_f32 %0, %2, %0\n\tv_accvgpr_write_b32 a10, %0\n\t"
        "v_accvgpr_read_b32 %1, a11\n\tv_mul_f32 %1, %2, %1\n\tv_accvgpr_write_b32 a11, %1\n\t"
        "v_accvgpr_read_b32 %0, a12\n\tv_mul_f32 %0, %2, %0\n\tv_accvgpr_write_b32 a12, %0\n\t"
        "v_accvgpr_read_b32 %1, a13\n\tv_mul_f32 %1, %2, %1\n\tv_accvgpr_write_b32 a13, %1\n\t"
        "v_accvgpr_read_b32 %0, a14\n\tv_mul_f32 %0, %2, %0\n\tv_accvgpr_write_b32 a14, %0\n\t"
        "v_accvgpr_read_b32 %1, a15\n\tv_mul_f32 %1, %2, %1\n\tv_accvgpr_write_b32 a15, %1\n\t"
        "v_accvgpr_read_b32 %0, a16\n\tv_mul_f32 %0, %3, %0\n\tv_accvgpr_write_b32 a16, %0\n\t"
        "v_accvgpr_read_b32 %1, a17\n\tv_mul_f32 %1, %3, %1\n\tv_accvgpr_write_b32 a17, %1\n\t"
        "v_accvgpr_read_b32 %0, a18\n\tv_mul_f32 %0, %3, %0\n\tv_accvgpr_write_b32 a18, %0\n\t"
        "v_accvgpr_read_b32 %1, a19\n\tv_mul_f32 %1, %3, %1\n\tv_accvgpr_write_b32 a19, %1\n\t"
        "v_accvgpr_read_b32 %0, a20\n\tv_mul_f32 %0, %3, %0\n\tv_accvgpr_write_b32 a20, %0\n\t"
        "v_accvgpr_read_b32 %1, a21\n\tv_mul_f32 %1, %3, %1\n\tv_accvgpr_write_b32 a21, %1\n\t"
        "v_accvgpr_read_b32 %0, a22\n\tv_mul_f32 %0, %3, %0\n\tv_accvgpr_write_b32 a22, %0\n\t"
        "v_accvgpr_read_b32 %1, a23\n\tv_mul_f32 %1, %3, %1\n\tv_accvgpr_write_b32 a23, %1\n\t"
        "v_accvgpr_read_b32 %0, a24\n\tv_mul_f32 %0, %3, %0\n\tv_accvgpr_write_b32 a24, %0\n\t"
        "v_accvgpr_read_b32 %1, a25\n\tv_mul_f32 %1, %3, %1\n\tv_accvgpr_write_b32 a25, %1\n\t"
        "v_accvgpr_read_b32 %0, a26\n\tv_mul_f32 %0, %3, %0\n\tv_accvgpr_write_b32 a26, %0\n\t"
        "v_accvgpr_read_b32 %1, a27\n\tv_mul_f32 %1, %3, %1\n\tv_accvgpr_write_b32 a27, %1\n\t"
        "v_accvgpr_read_b32 %0, a28\n\tv_mul_f32 %0, %3, %0\n\tv_accvgpr_write_b32 a28, %0\n\t"
        "v_accvgpr_read_b32 %1, a29\n\tv_mul_f32 %1, %3, %1\n\tv_accvgpr_write_b32 a29, %1\n\t"
        "v_accvgpr_read_b32 %0, a30\n\tv_mul_f32 %0, %3, %0\n\tv_accvgpr_write_b32 a30, %0\n\t"
        "v_accvgpr_read_b32 %1, a31\n\tv_mul_f32 %1, %3, %1\n\tv_accvgpr_write_b32 a31, %1\n\t"
        "v_accvgpr_read_b32 %0, a32\n\tv_mul_f32 %0, %2, %0\n\tv_accvgpr_write_b32 a32, %0\n\t"
        "v_accvgpr_read_b32 %1, a33\n\tv_mul_f32 %1, %2, %1\n\tv_accvgpr_write_b32 a33, %1\n\t"
        "v_accvgpr_read_b32 %0, a34\n\tv_mul_f32 %0, %2, %0\n\tv_accvgpr_write_b32 a34, %0\n\t"
        "v_accvgpr_read_b32 %1, a35\n\tv_mul_f32 %1, %2, %1\n\tv_accvgpr_write_b32 a35, %1\n\t"
        "v_accvgpr_read_b32 %0, a36\n\tv_mul_f32 %0, %2, %0\n\tv_accvgpr_write_b32 a36, %0\n\t"
        "v_accvgpr_read_b32 %1, a37\n\tv_mul_f32 %1, %2, %1\n\tv_accvgpr_write_b32 a37, %1\n\t"
        "v_accvgpr_read_b32 %0, a38\n\tv_mul_f32 %0, %2, %0\n\tv_accvgpr_write_b32 a38, %0\n\t"
        "v_accvgpr_read_b32 %1, a39\n\tv_mul_f32 %1, %2, %1\n\tv_accvgpr_write_b32 a39, %1\n\t"
        "v_accvgpr_read_b32 %0, a40\n\tv_mul_f32 %0, %2, %0\n\tv_accvgpr_write_b32 a40, %0\n\t"
        "v_accvgpr_read_b32 %1, a41\n\tv_mul_f32 %1, %2, %1\n\tv_accvgpr_write_b32 a41, %1\n\t"
        "v_accvgpr_read_b32 %0, a42\n\tv_mul_f32 %0, %2, %0\n\tv_accvgpr_write_b32 a42, %0\n\t"
        "v_accvgpr_read_b32 %1, a43\n\tv_mul_f32 %1, %2, %1\n\tv_accvgpr_write_b32 a43, %1\n\t"
        "v_accvgpr_read_b32 %0, a44\n\tv_mul_f32 %0, %2, %0\n\tv_accvgpr_write_b32 a44, %0\n\t"
        "v_accvgpr_read_b32 %1, a45\n\tv_mul_f32 %1, %2, %1\n\tv_accvgpr_write_b32 a45, %1\n\t"
        "v_accvgpr_read_b32 %0, a46\n\tv_mul_f32 %0, %2, %0\n\tv_accvgpr_write_b32 a46, %0\n\t"
        "v_accvgpr_read_b32 %1, a47\n\tv_mul_f32 %1, %2, %1\n\tv_accvgpr_write_b32 a47, %1\n\t"
        "v_accvgpr_read_b32 %0, a48\n\tv_mul_f32 %0, %3, %0\n\tv_accvgpr_write_b32 a48, %0\n\t"
        "v_accvgpr_read_b32 %1, a49\n\tv_mul_f32 %1, %3, %1\n\tv_accvgpr_write_b32 a49, %1\n\t"
        "v_accvgpr_read_b32 %0, a50\n\tv_mul_f32 %0, %3, %0\n\tv_accvgpr_write_b32 a50, %0\n\t"
        "v_accvgpr_read_b32 %1, a51\n\tv_mul_f32 %1, %3, %1\n\tv_accvgpr_write_b32 a51, %1\n\t"
        "v_accvgpr_read_b32 %0, a52\n\tv_mul_f32 %0, %3, %0\n\tv_accvgpr_write_b32 a52, %0\n\t"
        "v_accvgpr_read_b32 %1, a53\n\tv_mul_f32 %1, %3, %1\n\tv_accvgpr_write_b32 a53, %1\n\t"
        "v_accvgpr_read_b32 %0, a54\n\tv_mul_f32 %0, %3, %0\n\tv_accvgpr_write_b32 a54, %0\n\t"
        "v_accvgpr_read_b32 %1, a55\n\tv_mul_f32 %1, %3, %1\n\tv_accvgpr_write_b32 a55, %1\n\t"
        "v_accvgpr_read_b32 %0, a56\n\tv_mul_f32 %0, %3, %0\n\tv_accvgpr_write_b32 a56, %0\n\t"
        "v_accvgpr_read_b32 %1, a57\n\tv_mul_f32 %1, %3, %1\n\tv_accvgpr_write_b32 a57, %1\n\t"
        "v_accvgpr_read_b32 %0, a58\n\tv_mul_f32 %0, %3, %0\n\tv_accvgpr_write_b32 a58, %0\n\t"
        "v_accvgpr_read_b32 %1, a59\n\tv_mul_f32 %1, %3, %1\n\tv_accvgpr_write_b32 a59, %1\n\t"
        "v_accvgpr_read_b32 %0, a60\n\tv_mul_f32 %0, %3, %0\n\tv_accvgpr_write_b32 a60, %0\n\t"
        "v_accvgpr_read_b32 %1, a61\n\tv_mul_f32 %1, %3, %1\n\tv_accvgpr_write_b32 a61, %1\n\t"
        "v_accvgpr_read_b32 %0, a62\n\tv_mul_f32 %0, %3, %0\n\tv_accvgpr_write_b32 a62, %0\n\t"
        "v_accvgpr_read_b32 %1, a63\n\tv_mul_f32 %1, %3, %1\n\tv_accvgpr_write_b32 a63, %1\n\t"
        "v_accvgpr_read_b32 %0, a64\n\tv_mul_f32 %0, %2, %0\n\tv_accvgpr_write_b32 a64, %0\n\t"
        "v_accvgpr_read_b32 %1, a65\n\tv_mul_f32 %1, %2, %1\n\tv_accvgpr_write_b32 a65, %1\n\t"
        "v_accvgpr_read_b32 %0, a66\n\tv_mul_f32 %0, %2, %0\n\tv_accvgpr_write_b32 a66, %0\n\t"
        "v_accvgpr_read_b32 %1, a67\n\tv_mul_f32 %1, %2, %1\n\tv_accvgpr_write_b32 a67, %1\n\t"
        "v_accvgpr_read_b32 %0, a68\n\tv_mul_f32 %0, %2, %0\n\tv_accvgpr_write_b32 a68, %0\n\t"
        "v_accvgpr_read_b32 %1, a69\n\tv_mul_f32 %1, %2, %1\n\tv_accvgpr_write_b32 a69, %1\n\t"
        "v_accvgpr_read_b32 %0, a70\n\tv_mul_f32 %0, %2, %0\n\tv_accvgpr_write_b32 a70, %0\n\t"
        "v_accvgpr_read_b32 %1, a71\n\tv_mul_f32 %1, %2, %1\n\tv_accvgpr_write_b32 a71, %1\n\t"
        "v_accvgpr_read_b32 %0, a72\n\tv_mul_f32 %0, %2, %0\n\tv_accvgpr_write_b32 a72, %0\n\t"
        "v_accvgpr_read_b32 %1, a73\n\tv_mul_f32 %1, %2, %1\n\tv_accvgpr_write_b32 a73, %1\n\t"
        "v_accvgpr_read_b32 %0, a74\n\tv_mul_f32 %0, %2, %0\n\tv_accvgpr_write_b32 a74, %0\n\t"
        "v_accvgpr_read_b32 %1, a75\n\tv_mul_f32 %1, %2, %1\n\tv_accvgpr_write_b32 a75, %1\n\t"
        "v_accvgpr_read_b32 %0, a76\n\tv_mul_f32 %0, %2, %0\n\tv_accvgpr_write_b32 a76, %0\n\t"
        "v_accvgpr_read_b32 %1, a77\n\tv_mul_f32 %1, %2, %1\n\tv_accvgpr_write_b32 a77, %1\n\t"
        "v_accvgpr_read_b32 %0, a78\n\tv_mul_f32 %0, %2, %0\n\tv_accvgpr_write_b32 a78, %0\n\t"
        "v_accvgpr_read_b32 %1, a79\n\tv_mul_f32 %1, %2, %1\n\tv_accvgpr_write_b32 a79, %1\n\t"
        "v_accvgpr_read_b32 %0, a80\n\tv_mul_f32 %0, %3, %0\n\tv_accvgpr_write_b32 a80, %0\n\t"
        "v_accvgpr_read_b32 %1, a81\n\tv_mul_f32 %1, %3, %1\n\tv_accvgpr_write_b32 a81, %1\n\t"
        "v_accvgpr_read_b32 %0, a82\n\tv_mul_f32 %0, %3, %0\n\tv_accvgpr_write_b32 a82, %0\n\t"
        "v_accvgpr_read_b32 %1, a83\n\tv_mul_f32 %1, %3, %1\n\tv_accvgpr_write_b32 a83, %1\n\t"
        "v_accvgpr_read_b32 %0, a84\n\tv_mul_f32 %0, %3, %0\n\tv_accvgpr_write_b32 a84, %0\n\t"
        "v_accvgpr_read_b32 %1, a85\n\tv_mul_f32 %1, %3, %1\n\tv_accvgpr_write_b32 a85, %1\n\t"
        "v_accvgpr_read_b32 %0, a86\n\tv_mul_f32 %0, %3, %0\n\tv_accvgpr_write_b32 a86, %0\n\t"
        "v_accvgpr_read_b32 %1, a87\n\tv_mul_f32 %1, %3, %1\n\tv_accvgpr_write_b32 a87, %1\n\t"
        "v_accvgpr_read_b32 %0, a88\n\tv_mul_f32 %0, %3, %0\n\tv_accvgpr_write_b32 a88, %0\n\t"
        "v_accvgpr_read_b32 %1, a89\n\tv_mul_f32 %1, %3, %1\n\tv_accvgpr_write_b32 a89, %1\n\t"
        "v_accvgpr_read_b32 %0, a90\n\tv_mul_f32 %0, %3, %0\n\tv_accvgpr_write_b32 a90, %0\n\t"
        "v_accvgpr_read_b32 %1, a91\n\tv_mul_f32 %1, %3, %1\n\tv_accvgpr_write_b32 a91, %1\n\t"
        "v_accvgpr_read_b32 %0, a92\n\tv_mul_f32 %0, %3, %0\n\tv_accvgpr_write_b32 a92, %0\n\t"
        "v_accvgpr_read_b32 %1, a93\n\tv_mul_f32 %1, %3, %1\n\tv_accvgpr_write_b32 a93, %1\n\t"
        "v_accvgpr_read_b32 %0, a94\n\tv_mul_f32 %0, %3, %0\n\tv_accvgpr_write_b32 a94, %0\n\t"
        "v_accvgpr_read_b32 %1, a95\n\tv_mul_f32 %1, %3, %1\n\tv_accvgpr_write_b32 a95, %1\n\t"
        "v_accvgpr_read_b32 %0, a96\n\tv_mul_f32 %0, %2, %0\n\tv_accvgpr_write_b32 a96, %0\n\t"
        "v_accvgpr_read_b32 %1, a97\n\tv_mul_f32 %1, %2, %1\n\tv_accvgpr_write_b32 a97, %1\n\t"
        "v_accvgpr_read_b32 %0, a98\n\tv_mul_f32 %0, %2, %0\n\tv_accvgpr_write_b32 a98, %0\n\t"
        "v_accvgpr_read_b32 %1, a99\n\tv_mul_f32 %1, %2, %1\n\tv_accvgpr_write_b32 a99, %1\n\t"
        "v_accvgpr_read_b32 %0, a100\n\tv_mul_f32 %0, %2, %0\n\tv_accvgpr_write_b32 a100, %0\n\t"
        "v_accvgpr_read_b32 %1, a101\n\tv_mul_f32 %1, %2, %1\n\tv_accvgpr_write_b32 a101, %1\n\t"
        "v_accvgpr_read_b32 %0, a102\n\tv_mul_f32 %0, %2, %0\n\tv_accvgpr_write_b32 a102, %0\n\t"
        "v_accvgpr_read_b32 %1, a103\n\tv_mul_f32 %1, %2, %1\n\tv_accvgpr_write_b32 a103, %1\n\t"
        "v_accvgpr_read_b32 %0, a104\n\tv_mul_f32 %0, %2, %0\n\tv_accvgpr_write_b32 a104, %0\n\t"
        "v_accvgpr_read_b32 %1, a105\n\tv_mul_f32 %1, %2, %1\n\tv_accvgpr_write_b32 a105, %1\n\t"
        "v_accvgpr_read_b32 %0, a106\n\tv_mul_f32 %0, %2, %0\n\tv_accvgpr_write_b32 a106, %0\n\t"
        "v_accvgpr_read_b32 %1, a107\n\tv_mul_f32 %1, %2, %1\n\tv_accvgpr_write_b32 a107, %1\n\t"
        "v_accvgpr_read_b32 %0, a108\n\tv_mul_f32 %0, %2, %0\n\tv_accvgpr_write_b32 a108, %0\n\t"
        "v_accvgpr_read_b32 %1, a109\n\tv_mul_f32 %1, %2, %1\n\tv_accvgpr_write_b32 a109, %1\n\t"
        "v_accvgpr_read_b32 %0, a110\n\tv_mul_f32 %0, %2, %0\n\tv_accvgpr_write_b32 a110, %0\n\t"
        "v_accvgpr_read_b32 %1, a111\n\tv_mul_f32 %1, %2, %1\n\tv_accvgpr_write_b32 a111, %1\n\t"
        "v_accvgpr_read_b32 %0, a112\n\tv_mul_f32 %0, %3, %0\n\tv_accvgpr_write_b32 a112, %0\n\t"
        "v_accvgpr_read_b32 %1, a113\n\tv_mul_f32 %1, %3, %1\n\tv_accvgpr_write_b32 a113, %1\n\t"
        "v_accvgpr_read_b32 %0, a114\n\tv_mul_f32 %0, %3, %0\n\tv_accvgpr_write_b32 a114, %0\n\t"
        "v_accvgpr_read_b32 %1, a115\n\tv_mul_f32 %1, %3, %1\n\tv_accvgpr_write_b32 a115, %1\n\t"
        "v_accvgpr_read_b32 %0, a116\n\tv_mul_f32 %0, %3, %0\n\tv_accvgpr_write_b32 a116, %0\n\t"
        "v_accvgpr_read_b32 %1, a117\n\tv_mul_f32 %1, %3, %1\n\tv_accvgpr_write_b32 a117, %1\n\t"
        "v_accvgpr_read_b32 %0, a118\n\tv_mul_f32 %0, %3, %0\n\tv_accvgpr_write_b32 a118, %0\n\t"
        "v_accvgpr_read_b32 %1, a119\n\tv_mul_f32 %1, %3, %1\n\tv_accvgpr_write_b32 a119, %1\n\t"
        "v_accvgpr_read_b32 %0, a120\n\tv_mul_f32 %0, %3, %0\n\tv_accvgpr_write_b32 a120, %0\n\t"
        "v_accvgpr_read_b32 %1, a121\n\tv_mul_f32 %1, %3, %1\n\tv_accvgpr_write_b32 a121, %1\n\t"
        "v_accvgpr_read_b32 %0, a122\n\tv_mul_f32 %0, %3, %0\n\tv_accvgpr_write_b32 a122, %0\n\t"
        "v_accvgpr_read_b32 %1, a123\n\tv_mul_f32 %1, %3, %1\n\tv_accvgpr_write_b32 a123, %1\n\t"
        "v_accvgpr_read_b32 %0, a124\n\tv_mul_f32 %0, %3, %0\n\tv_accvgpr_write_b32 a124, %0\n\t"
        "v_accvgpr_read_b32 %1, a125\n\tv_mul_f32 %1, %3, %1\n\tv_accvgpr_write_b32 a125, %1\n\t"
        "v_accvgpr_read_b32 %0, a126\n\tv_mul_f32 %0, %3, %0\n\tv_accvgpr_write_b32 a126, %0\n\t"
        "v_accvgpr_read_b32 %1, a127\n\tv_mul_f32 %1, %3, %1\n\tv_accvgpr_write_b32 a127, %1\n\t"
        "s_nop 1" : "=&v"(t0), "=&v"(t1) : "v"(c.fct[0]), "v"(c.fct[1]) : W4_ACLOB);
      c.lsum[0] = fmul_s(c.lsum[0], c.fct[0]); c.lsum[1] = fmul_s(c.lsum[1], c.fct[1]); c.pend = false;
    }
  }
  asm volatile("s_waitcnt lgkmcnt(0)" ::: "memory");
  GAP();
}
template <int X, bool PEEL, bool ZERO1, int DK = 1, int DV = 1, bool KR = true> DI void phaseB(W4C& c, const int t) { phaseB_<X, PEEL, ZERO1, DK, DV, KR>(c, t, make_iseq<32>{}); }

template <int... Is> DI void q_reads(W4C& c, unsigned qoff, iseq<Is...>) { (q_read<Is>(c, qoff), ...); }
template <int... Is> DI void k_reads(W4C& c, unsigned slot, iseq<Is...>) { (k_read<Is>(c, slot), ...); }
template <int... Is> DI void tr_reads(W4C& c, unsigned vslot, iseq<Is...>) { (tr_read<Is>(c, vslot), ...); }
template <int X, int... Cs> DI void packs(W4C& c, iseq<Cs...>) { (pack1<X, ((Cs >> 3) & 1), 2 * (Cs >> 4) + ((Cs >> 2) & 1), (Cs & 3)>(c), ...); }
template <int X, int J> DI void rowsum1_padded(W4C& c) { rowsum1<X, J>(c); asm volatile("s_nop 1"); }
template <int X, int... Js> DI void rowsums(W4C& c, iseq<Js...>) { (rowsum1_padded<X, Js>(c), ...); }
template <bool Z, int... Ms> DI void pv_all(W4C& c, iseq<Ms...>) { (pv_mfma<Ms, Z>(c), ...); }

typedef __attribute__((address_space(3))) bf16x4* lds_b4p;
typedef __attribute__((address_space(3))) bf16x8* lds_b8p;
typedef __attribute__((address_space(3))) char*   lds_cp;
DI bf16x4 ldtr(unsigned addr) { return __builtin_amdgcn_ds_read_tr16_b64_v4bf16((lds_b4p)(size_t)addr); }
DI bf16x8 ld128(unsigned addr) { return *(lds_b8p)(size_t)addr; }
DI void dma16(unsigned m0v, unsigned voff, __amdgpu_buffer_rsrc_t srd, unsigned soff) {
  asm volatile("s_mov_b32 m0, %0\n\ts_nop 0\n\tbuffer_load_dwordx4 %1, %2, %3 offen lds" :: "s"(m0v), "v"(voff), "s"(srd), "s"(soff) : "m0", "memory");
}
DI void dma16n(unsigned m0v, unsigned voff, __amdgpu_buffer_rsrc_t srd, unsigned soff) {
  asm volatile("s_nop 4\n\ts_mov_b32 m0, %0\n\ts_nop 0\n\tbuffer_load_dwordx4 %1, %2, %3 offen lds" :: "s"(m0v), "v"(voff), "s"(srd), "s"(soff) : "m0", "memory");
}

DI void touch64(unsigned m0v, unsigned voff, __amdgpu_buffer_rsrc_t srd, unsigned soff) {
  asm volatile("s_nop 4\n\ts_mov_b32 m0, %0\n\ts_nop 0\n\tbuffer_load_dword %1, %2, %3 offen lds" :: "s"(m0v), "v"(voff), "s"(srd), "s"(soff) : "m0", "memory");
}

DI void touch2(unsigned m0v, const unsigned short* base, unsigned nbytes, unsigned soff) {
  __amdgpu_buffer_rsrc_t srd = __builtin_amdgcn_make_buffer_rsrc((void*)base, (short)0, nbytes, SRD_WORD3);
  unsigned vo;
  asm volatile("v_mbcnt_lo_u32_b32 %0, -1, 0\n\tv_mbcnt_hi_u32_b32 %0, -1, %0\n\tv_lshl_add_u32 %0, %0, 6, %3\n\ts_mov_b32 m0, %1\n\ts_nop 0\n\tbuffer_load_dword %0, %2, 0 offen lds\n\t"
               "v_add_u32 %0, 0x400000, %0\n\tbuffer_load_dword %0, %2, 0 offen lds" : "=&v"(vo) : "s"(m0v), "s"(srd), "s"(soff) : "m0", "memory");
}

struct QP {
  unsigned vxsrc[2], vwsrc, vxb[4], vwb;
  unsigned m0x, m0w, sx, sw;
  __amdgpu_buffer_rsrc_t srdX, srdW;
  bf16x8 W[2][4][4];
  bf16x8 X[2][2][4];
};
constexpr unsigned QSTG(int c) { return 49152u * (unsigned)((c + 2) % 3); }
template <int C, int P, bool PAD> DI void qp_dma(QP& q) {
  if constexpr (P < 8) {
    const unsigned m0v = q.m0x + QSTG(C) + 1024u * P, so = q.sx + 8u * P * 1280u + 128u * C;
    if constexpr (PAD) dma16n(m0v, q.vxsrc[P & 1], q.srdX, so); else dma16(m0v, q.vxsrc[P & 1], q.srdX, so);
  } else {
    constexpr int p = P - 8;
    const unsigned m0v = q.m0w + QSTG(C) + 1024u * p, so = q.sw + (64u * C + 8u * (p >> 1)) * 8192u + 128u * (p & 1);
    if constexpr (PAD) dma16n(m0v, q.vwsrc, q.srdW, so); else dma16(m0v, q.vwsrc, q.srdW, so);
  }
}
template <int C, int... Ps> DI void qp_dma_all(QP& q, iseq<Ps...>) { (qp_dma<C, Ps, true>(q), ...); }
template <int C, int I> DI void qp_read(QP& q) {
  if constexpr (I < 16) {
    constexpr int ks = I >> 2, db = I & 3;
    const unsigned a = q.vwb + QSTG(C) + 4096u * ks + 512u * db;
    const bf16x4 lo = ldtr(a), up = ldtr(a + 256u);
    q.W[C & 1][db][ks] = __builtin_shufflevector(lo, up, 0, 1, 2, 3, 4, 5, 6, 7);
  } else {
    constexpr int ks = (I - 16) >> 1, qb = I & 1;
    q.X[C & 1][qb][ks] = ld128(q.vxb[ks] + QSTG(C) + 4096u * qb);
  }
}
template <int C, int... Is> DI void qp_read_all(QP& q, iseq<Is...>) { (qp_read<C, Is>(q), ...); }
template <int C, int M> DI void qp_gap(QP& q, W4C& c) {
  constexpr int ks = M >> 3, db = (M >> 1) & 3, qb = M & 1, ao = AO_(db, qb);
  if constexpr (C == 0 && ks == 0) asm volatile("s_nop 1\n\tv_mfma_f32_32x32x16_bf16 a[%c2:%c3], %0, %1, 0" :: "v"(q.W[C & 1][db][ks]), "v"(q.X[C & 1][qb][ks]), "i"(ao), "i"(ao + 15) : W4_ACLOB);
  else                             asm volatile("s_nop 1\n\tv_mfma_f32_32x32x16_bf16 a[%c2:%c3], %0, %1, a[%c2:%c3]" :: "v"(q.W[C & 1][db][ks]), "v"(q.X[C & 1][qb][ks]), "i"(ao), "i"(ao + 15) : W4_ACLOB);
  if constexpr (M < 24 && (M & 1) == 0) {
    constexpr int P = M >> 1;
    if constexpr (C + 3 < 10) qp_dma<C + 3, P, false>(q);
    else if constexpr (C == 7) {
      if constexpr (P < 4)      dma16(c.m0k + 1024u * P, c.vksrc[P & 1], c.srdK, c.sofk + KRS * 4u * P);
      else if constexpr (P < 8) dma16(c.m0v + 1024u * (P - 4), c.vvsrc, c.srdV, c.sofv + KRS * 8u * ((P - 4) >> 1) + 128u * ((P - 4) & 1));
      else                      dma16(c.m0k + 0x4000u + 1024u * (P - 8), c.vksrc[P & 1], c.srdK, KTILEB + c.sofk + KRS * 4u * (P - 8));
    }
  }
  if constexpr (C + 1 < 10) {
    if constexpr (M < 24) {
      constexpr int rks = M / 6, r = M % 6;
      if constexpr (r < 2) qp_read<C + 1, 16 + 2 * rks + r>(q); else qp_read<C + 1, 4 * rks + (r - 2)>(q);
    }
  }
  GAP();
}
template <int C, int... Ms> DI void qp_chunk_(QP& q, W4C& c, iseq<Ms...>) {
  if constexpr (C + 1 < 10) asm volatile("s_waitcnt vmcnt(12) lgkmcnt(0)\n\ts_barrier" ::: "memory");
  GAP();
  (qp_gap<C, Ms>(q, c), ...);
}
template <int... Cs> DI void qp_chunks(QP& q, W4C& c, iseq<Cs...>) { (qp_chunk_<Cs>(q, c, make_iseq<32>{}), ...); }

extern "C" __global__ __launch_bounds__(256, 1)
void k2_attn(const unsigned short* __restrict__ XN, const unsigned short* __restrict__ WQ, const unsigned short* __restrict__ Kc, const unsigned short* __restrict__ Vc,
             const unsigned short* __restrict__ Z2, unsigned short* __restrict__ Z2N, unsigned short* __restrict__ O,
             const unsigned short* __restrict__ WSP, const unsigned short* __restrict__ WO) {
  extern __shared__ __attribute__((aligned(16))) char lds[];
  const unsigned tid = threadIdx.x, lane = tid & 63u, wid = rfl(tid >> 6), l32 = lane & 31u, hi = lane >> 5;
  constexpr int T = NT;
  const unsigned bid = blockIdx.x, xcd = bid & 7u, jj = bid >> 3;
  const unsigned h = xcd * 4u + (jj & 3u), y = jj >> 2;
  const unsigned row0 = 256u * y + 64u * wid;
  STAMP(1, 0);
  u32x4 zv[4];
  #pragma unroll
  for (int i = 0; i < 4; ++i) { const unsigned pl = 2u * i + hi, pi = 7u * wid + pl; zv[i] = (u32x4){0u, 0u, 0u, 0u};
    if (pl < 7u && pi < 27u) zv[i] = *(const u32x4*)((const char*)Z2 + (((bid >> 1) * 54u + 27u + pi) * 64u + ((lane >> 3) & 3u) * 16u + 8u * (bid & 1u) + (lane & 7u)) * 16u); }

  W4C c;
  c.T = T; c.THR = THR;
  c.ldsb = (unsigned)(size_t)lds;
  #pragma unroll
  for (int j = 0; j < 4; ++j) c.vkb[j] = c.ldsb + KRING + 256u * l32 + 16u * ((2u * j + hi) ^ (l32 & 7u));
  c.vvb = c.ldsb + VRING + (((lane & 3u) << 3) | (((lane >> 2) & 3u) << 6) | (((lane >> 4) & 1u) << 5) | (hi << 8));
  #pragma unroll
  for (int par = 0; par < 2; ++par) c.vksrc[par] = KRS * (lane >> 4) + 16u * ((lane & 15u) ^ (4u * par + (lane >> 4)));
  c.vvsrc = KRS * ((lane >> 2) & 7u) + 64u * hi + 16u * (lane & 3u);
  c.m0k = rfl(c.ldsb + KRING + 4096u * wid); c.m0v = rfl(c.ldsb + VRING + 4096u * wid);
  c.sofk = rfl(KRS * 16u * wid); c.sofv = rfl(KRS * 16u * wid);
  c.srdK = __builtin_amdgcn_make_buffer_rsrc((void*)((const char*)Kc + (size_t)h * (SEQ * 256)), (short)0, SEQ * 256, SRD_WORD3);
  c.srdV = __builtin_amdgcn_make_buffer_rsrc((void*)((const char*)Vc + (size_t)h * (SEQ * 256)), (short)0, SEQ * 256, SRD_WORD3);
  c.srdKn = c.srdK; c.srdVn = c.srdV; c.srdQn = c.srdK; c.qsofn = 0; c.qstepn = 0; c.m0q = 0;
  {
    QP q;
    #pragma unroll
    for (int par = 0; par < 2; ++par) q.vxsrc[par] = 1280u * (lane >> 3) + 16u * ((lane & 7u) ^ (4u * par + (lane >> 4)));
    q.vwsrc = 8192u * ((lane >> 2) & 7u) + 64u * hi + 16u * (lane & 3u);
    #pragma unroll
    for (int ks = 0; ks < 4; ++ks) q.vxb[ks] = c.ldsb + 8192u * wid + 128u * l32 + 16u * ((2u * ks + hi) ^ ((l32 >> 1) & 7u));
    q.vwb = c.ldsb + 32768u + 2048u * hi + 64u * ((lane >> 2) & 3u) + 32u * ((lane >> 4) & 1u) + 8u * (lane & 3u);
    q.m0x = rfl(c.ldsb + 8192u * wid); q.m0w = rfl(c.ldsb + 32768u + 4096u * wid);
    q.sx = rfl(row0 * 1280u); q.sw = rfl(16u * wid * 8192u + h * 256u);
    q.srdX = __builtin_amdgcn_make_buffer_rsrc((void*)XN, (short)0, SEQ * DM * 2, SRD_WORD3);
    q.srdW = __builtin_amdgcn_make_buffer_rsrc((void*)WQ, (short)0, DM * HD * 2, SRD_WORD3);
    qp_dma_all<0>(q, make_iseq<12>{}); qp_dma_all<1>(q, make_iseq<12>{}); qp_dma_all<2>(q, make_iseq<12>{});
    asm volatile("s_waitcnt vmcnt(24)\n\ts_barrier" ::: "memory");
    STAMP(1, 1);
    qp_read_all<0>(q, make_iseq<24>{});
    qp_chunks(q, c, make_iseq<10>{});
  }
  STAMP(1, 2);
  {
    const float nw = wid < 3u ? 224.f : 192.f;
    float sum = 0.f;
    #pragma unroll
    for (int i = 0; i < 4; ++i)
      #pragma unroll
      for (int e = 0; e < 4; ++e) sum += __uint_as_float(zv[i][e] << 16) + __uint_as_float(zv[i][e] & 0xffff0000u);
    sum += __shfl_xor(sum, 8, 64); sum += __shfl_xor(sum, 16, 64); sum += __shfl_xor(sum, 32, 64);
    const float mw = sum / nw;
    float sq = 0.f;
    #pragma unroll
    for (int i = 0; i < 4; ++i) { if (i < 3 || (wid < 3u && hi == 0u)) {
      #pragma unroll
      for (int e = 0; e < 4; ++e) { const float a = __uint_as_float(zv[i][e] << 16) - mw, b = __uint_as_float(zv[i][e] & 0xffff0000u) - mw; sq += a * a + b * b; } } }
    sq += __shfl_xor(sq, 8, 64); sq += __shfl_xor(sq, 16, 64); sq += __shfl_xor(sq, 32, 64);
    if (lane < 8u) *(f32x2*)(lds + 147456u + 32u * lane + 8u * wid) = (f32x2){mw, sq};
  }
  asm volatile("s_nop 7\n\ts_nop 7" ::: "memory");
  #pragma unroll
  for (int qb = 0; qb < 2; ++qb)
    #pragma unroll
    for (int db = 0; db < 4; ++db) {
      float o[16];
      W4_READ_O(db, qb, o);
      #pragma unroll
      for (int s = 0; s < 2; ++s) {
        const unsigned a0 = cvtpk(o[8 * s + 0], o[8 * s + 1]), a1 = cvtpk(o[8 * s + 2], o[8 * s + 3]);
        const unsigned b0 = cvtpk(o[8 * s + 4], o[8 * s + 5]), b1 = cvtpk(o[8 * s + 6], o[8 * s + 7]);
        auto r0 = __builtin_amdgcn_permlane32_swap(a0, b0, false, false);
        auto r1 = __builtin_amdgcn_permlane32_swap(a1, b1, false, false);
        u32x4 w = {r0[0], r1[0], r0[1], r1[1]};
        q_write(qb, 2 * db + s, w);
      }
    }
  asm volatile("s_waitcnt vmcnt(0) lgkmcnt(0)\n\ts_barrier" ::: "memory");
  {
    const f32x4 p0 = *(const f32x4*)(lds + 147456u + 32u * (lane & 7u)), p1 = *(const f32x4*)(lds + 147456u + 32u * (lane & 7u) + 16u);
    const float mean = (224.f * (p0[0] + p0[2] + p1[0]) + 192.f * p1[2]) * (1.f / FH);
    const float d0 = p0[0] - mean, d1 = p0[2] - mean, d2 = p1[0] - mean, d3 = p1[2] - mean;
    const float m2 = (p0[1] + p0[3]) + (p1[1] + p1[3]) + 224.f * (d0 * d0 + d1 * d1 + d2 * d2) + 192.f * d3 * d3;
    const float rstd = 1.0f / sqrtf(m2 * (1.f / FH) + 1e-5f);
    #pragma unroll
    for (int i = 0; i < 8; ++i) { const unsigned pl = 2u * (i >> 1) + hi, bl = 14u * wid + 2u * pl + (i & 1); if (pl < 7u && bl < 54u) {
      const unsigned zct = bl / 9u, znb = bl - 9u * zct;
      u32x2 w;
      #pragma unroll
      for (int e = 0; e < 2; ++e) { const unsigned zz = zv[i >> 1][2 * (i & 1) + e]; w[e] = cvtpk((__uint_as_float(zz << 16) - mean) * rstd, (__uint_as_float(zz & 0xffff0000u) - mean) * rstd); }
      *(u32x2*)((char*)Z2N + ((zct * 32u + (bid >> 3)) * 72u + (bid & 7u) * 9u + znb) * 256u + ((lane & 7u) ^ ((bid & 1u) << 2)) * 32u + ((lane >> 3) & 3u) * 8u) = w; } }
  }
  k_reads(c, 0u, make_iseq<16>{});
  asm volatile("s_waitcnt lgkmcnt(0)\n\ts_nop 1" ::: "memory");

  STAMP(1, 3);
  c.mhat[0] = c.mhat[1] = 0.f; c.lsum[0] = c.lsum[1] = 0.f; c.fct[0] = c.fct[1] = 1.f; c.pend = false;
  phaseA<0, true>(c, 0);  phaseB<0, true,  false>(c, 0);
  touch2(c.ldsb + 148480u + 256u * wid, WSP, SEQ * SEQ * 2, 16384u * bid + 4096u * wid);
  phaseA<1, false, false, false, 2>(c, 1); phaseB<1, false, true>(c, 1);
  STAMP(1, 4);
  for (int t = 2; t < T - 2; t += 2) {
    phaseA<0, false>(c, t);     phaseB<0, false, false>(c, t);
    phaseA<1, false>(c, t + 1); phaseB<1, false, false>(c, t + 1);
  }
  STAMP(1, 5);
  phaseA<0, false>(c, T - 2); phaseB<0, false, false, 0, 1, true>(c, T - 2);
  phaseA<1, false>(c, T - 1); phaseB<1, false, false, 0, 0, false>(c, T - 1);
  touch2(c.ldsb + 148480u + 256u * wid, WO, HD * DM * 2, 16384u * bid + 4096u * wid);
  {
    const unsigned vslot = (unsigned)((T - 1) & 1) * 0x4000u;
    tr_reads(c, vslot, make_iseq<32>{});
    #pragma unroll
    for (int e = 0; e < 32; ++e) c.S[1][1][e >> 4][e & 15] = __builtin_amdgcn_exp2f(c.S[1][1][e >> 4][e & 15]);
    packs<1>(c, make_iseq<32>{});
    rowsums<1>(c, iseq<8, 9, 10, 11, 12, 13, 14, 15, 24, 25, 26, 27, 28, 29, 30, 31>{});
    #pragma unroll
    for (int qb = 0; qb < 2; ++qb) { const float rs = fadd_s(c.rs0[qb], c.rs1[qb]);
      auto pr = __builtin_amdgcn_permlane32_swap(__float_as_uint(rs), __float_as_uint(rs), false, false);
      c.lsum[qb] = fadd_s(c.lsum[qb], fadd_s(__uint_as_float(pr[0]), __uint_as_float(pr[1]))); }
    asm volatile("s_waitcnt lgkmcnt(0)" : W4_VLO :: "memory"); asm volatile("" : W4_VUP :: "memory");
    GAP();
    #pragma unroll
    for (int ks = 0; ks < 4; ++ks)
      #pragma unroll
      for (int db = 0; db < 4; ++db) c.Vf[db][ks] = __builtin_shufflevector(c.vlo[db][ks], c.vup[db][ks], 0, 1, 2, 3, 4, 5, 6, 7);
    asm volatile("s_nop 1" ::: "memory");
    pv_all<false>(c, make_iseq<32>{});
    asm volatile("s_nop 7\n\ts_nop 7" ::: "memory");
  }
  STAMP(1, 6);
  {
    unsigned lb; asm volatile("v_mbcnt_lo_u32_b32 %0, -1, 0\n\tv_mbcnt_hi_u32_b32 %0, -1, %0" : "=v"(lb));
    const unsigned l32b = lb & 31u, hib = lb >> 5;
    char* stg = lds + QSTAGE + 16384u * wid;
    #pragma unroll
    for (int qb = 0; qb < 2; ++qb) {
      const float il = 1.0f / c.lsum[qb];
      const unsigned r = 32u * qb + l32b;
      #pragma unroll
      for (int db = 0; db < 4; ++db) {
        float o[16];
        W4_READ_O(db, qb, o);
        #pragma unroll
        for (int g = 0; g < 4; ++g) {
          u32x2 w = {cvtpk(o[4 * g + 0] * il, o[4 * g + 1] * il), cvtpk(o[4 * g + 2] * il, o[4 * g + 3] * il)};
          *(u32x2*)(stg + 256u * r + 16u * ((4u * db + g) ^ (r & 15u)) + 8u * hib) = w;
        }
      }
    }
    asm volatile("s_waitcnt lgkmcnt(0)" ::: "memory");
    char* obase = (char*)O + (size_t)row0 * (HD * 2) + h * 256u;
    #pragma unroll
    for (int j = 0; j < 16; ++j) {
      const unsigned r = 4u * j + (lb >> 4), ch = lb & 15u;
      const u32x4 v = *(const u32x4*)(stg + 256u * r + 16u * (ch ^ (r & 15u)));
      *(u32x4*)(obase + (size_t)r * (HD * 2) + 16u * ch) = v;
    }
  }
  asm volatile("s_waitcnt vmcnt(0)" ::: "memory");
  STAMP(1, 7);
}

DI f32x4 mfma16(bf16x8 a, bf16x8 b, f32x4 c) { return __builtin_amdgcn_mfma_f32_16x16x32_bf16(a, b, c, 0, 0, 0); }
template <int NB> DI unsigned w_src(unsigned piece, unsigned lane, unsigned ldw) {
  const unsigned st = 4u * piece + (lane >> 4), kb = st / NB, nb = st - kb * NB;
  const unsigned rs = (lane >> 1) & 7u, r = rs ^ ((kb & 1u) << 2);
  return (8u * kb + r) * ldw + 32u * nb + 16u * (lane & 1u);
}
template <int NB> DI unsigned w_rd(unsigned lane) {
  const unsigned g = lane >> 4;
  return 256u * NB * g + 128u * (g & 1u) + 32u * ((lane >> 2) & 3u) + 8u * (lane & 3u);
}
DI bf16x8 w_frag(unsigned a0, unsigned a1, unsigned off) {
  const bf16x4 lo = ldtr(a0 + off), up = ldtr(a1 + off);
  return __builtin_shufflevector(lo, up, 0, 1, 2, 3, 4, 5, 6, 7);
}
DI float bflo(unsigned u) { return __uint_as_float(u << 16); }
DI float bfhi(unsigned u) { return __uint_as_float(u & 0xffff0000u); }
DI unsigned gelu2(f32x2 t) {
  const f32x2 arg = t * (t * t * -0.10294324f + -2.30220819f);
  const f32x2 d = (f32x2){__builtin_amdgcn_exp2f(arg[0]), __builtin_amdgcn_exp2f(arg[1])} + 1.0f;
  const f32x2 r = t * (f32x2){__builtin_amdgcn_rcpf(d[0]), __builtin_amdgcn_rcpf(d[1])};
  return cvtpk(r[0], r[1]);
}
DI float gelu_tanh(float x) {
  const float u = 0.7978845608028654f * (x + 0.044715f * x * x * x);
  return x * __builtin_amdgcn_rcpf(1.0f + __builtin_amdgcn_exp2f(-2.8853900817779268f * u));
}

DI unsigned wl_src(unsigned par, unsigned lane, unsigned ldw) {
  const unsigned rr = lane >> 3, f = ((rr >> 1) & 1u) | (par << 1);
  return rr * ldw + 32u * (((lane >> 1) & 3u) ^ f) + 16u * (lane & 1u);
}
DI unsigned wl_rd(unsigned ju, unsigned lane) {
  const unsigned g = lane >> 4, r4 = (lane >> 2) & 3u, f = (r4 >> 1) | ((g & 1u) << 1);
  return 128u * r4 + 32u * (ju ^ f) + 8u * (lane & 3u);
}
DI bf16x8 wl_frag(unsigned a) {
  const bf16x4 lo = ldtr(a), up = ldtr(a + 512u);
  return __builtin_shufflevector(lo, up, 0, 1, 2, 3, 4, 5, 6, 7);
}

template <int MODE> struct GC {
  static constexpr int MB = MODE == 2 ? 2 : 4, NB = MODE == 2 ? 9 : 4, NM = MB * NB + (MODE == 0 ? 6 : 0), P = MODE == 2 ? 9 : 8;
  static constexpr unsigned STG = MODE == 2 ? 34816u : 32768u;
  static constexpr unsigned LDX = MODE == 0 ? DM * 2 : MODE == 1 ? HD * 2 : SEQ * 2, LDW = MODE == 0 ? FF * 2 : DM * 2, WSTEP = MODE == 2 ? 18432u : 64u * LDW;
  f32x4 acc[MB][NB], cs[2], rs[2], rq[2];
  bf16x8 X[2][MB], W[2][NB], ones;
  unsigned xa[2][2][2], wr[2][4], xsrc[2], wsrc[2];
  unsigned m0x, m0w, m0e, sx, sw, swe;
  __amdgpu_buffer_rsrc_t srdX, srdW;
};
template <int MODE, int ST, int Q> DI void g_dmax(GC<MODE>& c) { dma16(c.m0x + GC<MODE>::STG * ST + 1024u * Q, c.xsrc[Q & 1], c.srdX, c.sx + 8u * Q * GC<MODE>::LDX); }
template <int MODE, int ST, int Q> DI void g_dmaw(GC<MODE>& c) {
  if constexpr (Q == 4) asm volatile("s_mov_b32 m0, %0\n\ts_mov_b32 exec_hi, 0\n\tbuffer_load_dwordx4 %1, %2, %3 offen lds\n\ts_mov_b32 exec_hi, -1"
                                     :: "s"(c.m0e + GC<MODE>::STG * ST), "v"(c.wsrc[0]), "s"(c.srdW), "s"(c.sw + c.swe) : "m0", "memory");
  else dma16(c.m0w + GC<MODE>::STG * ST + 1024u * Q, c.wsrc[Q >> 1], c.srdW, c.sw + (MODE == 2 ? 1024u * Q : 8u * (Q >> 1) * GC<MODE>::LDW + 128u * (Q & 1)));
}
template <int MODE, int ST, int KS, int U> DI void g_read(GC<MODE>& c) {
  constexpr int MB = GC<MODE>::MB, H = ST >> 1; constexpr unsigned sb = GC<MODE>::STG * (ST & 1);
  if constexpr (U < MB) c.X[KS][U] = ld128(c.xa[H][KS][U >> 1] + sb + 2048u * (U & 1));
  else { constexpr int nb = U - MB;
    if constexpr (MODE == 2) c.W[KS][nb] = nb < 8 ? w_frag(c.wr[H][0], c.wr[H][1], sb + 9216u * KS + 256u * nb) : w_frag(c.wr[H][2], c.wr[H][3], sb + 9216u * KS);
    else                     c.W[KS][nb] = wl_frag(c.wr[H][nb] + sb + 8192u * KS);
  }
}
template <int MODE, int ST, int KS, int... Us> DI void g_read_all(GC<MODE>& c, iseq<Us...>) { (g_read<MODE, ST, KS, Us>(c), ...); }
template <int MODE, int ST, int PH, bool RD, bool DMA_, int M> DI void g_gap(GC<MODE>& c) {
  constexpr int MB = GC<MODE>::MB, NB = GC<MODE>::NB, NU = MB + NB;
  if constexpr (M < MB * NB) asm volatile("v_mfma_f32_16x16x32_bf16 %0, %1, %2, %0" : "+v"(c.acc[M % MB][M / MB]) : "v"(c.W[PH][M / MB]), "v"(c.X[PH][M % MB]));
  else if constexpr (M < 18) asm volatile("v_mfma_f32_16x16x32_bf16 %0, %1, %2, %0" : "+v"(c.cs[M - 16]) : "v"(c.W[PH][M - 16]), "v"(c.ones));
  else if constexpr (M < 20) asm volatile("v_mfma_f32_16x16x32_bf16 %0, %1, %2, %0" : "+v"(c.rs[M - 18]) : "v"(c.ones), "v"(c.X[PH][M - 18]));
  else                       asm volatile("v_mfma_f32_16x16x32_bf16 %0, %1, %1, %0" : "+v"(c.rq[M - 20]) : "v"(c.X[PH][M - 20]));
  if constexpr (RD && M < NU) g_read<MODE, PH == 0 ? ST : (ST + 1) & 3, PH ^ 1, M>(c);
  if constexpr (DMA_ && M >= 8 && (M & 1) == (NU & 1)) {
    constexpr int q = (M - 8) >> 1;
    if constexpr (PH == 0) { if constexpr (q < GC<MODE>::P - 4) g_dmaw<MODE, (ST + 2) & 3, q>(c); }
    else                   { if constexpr (q < 4) g_dmax<MODE, (ST + 3) & 3, q>(c); }
  }
  GAP();
}
template <int MODE, int ST, int PH, bool RD, bool DMA_, int... Ms> DI void g_phase(GC<MODE>& c, iseq<Ms...>) { (g_gap<MODE, ST, PH, RD, DMA_, Ms>(c), ...); }
template <int MODE, int ST, int REM> DI void g_tile(GC<MODE>& c) {
  g_phase<MODE, ST, 0, true, (REM >= 2)>(c, make_iseq<GC<MODE>::NM>{});
  if constexpr (REM >= 2) c.sw += GC<MODE>::WSTEP;
  if constexpr (REM >= 2) { if constexpr (MODE == 2) asm volatile("s_waitcnt vmcnt(9)\n\ts_barrier" ::: "memory"); else asm volatile("s_waitcnt vmcnt(8)\n\ts_barrier" ::: "memory"); }
  else if constexpr (REM == 1) asm volatile("s_waitcnt vmcnt(0)\n\ts_barrier" ::: "memory");
  GAP();
  g_phase<MODE, ST, 1, (REM >= 1), (REM >= 3)>(c, make_iseq<GC<MODE>::NM>{});
  if constexpr (REM >= 3) c.sx += 128u;
}
template <int MODE, int ST> DI void g_issue_x(GC<MODE>& c) { g_dmax<MODE, ST, 0>(c); g_dmax<MODE, ST, 1>(c); g_dmax<MODE, ST, 2>(c); g_dmax<MODE, ST, 3>(c); c.sx += 128u; }
template <int MODE, int ST> DI void g_issue_w(GC<MODE>& c) { g_dmaw<MODE, ST, 0>(c); g_dmaw<MODE, ST, 1>(c); g_dmaw<MODE, ST, 2>(c); g_dmaw<MODE, ST, 3>(c); if constexpr (MODE == 2) g_dmaw<MODE, ST, 4>(c); c.sw += GC<MODE>::WSTEP; }
template <int MODE, int NTK> DI void g_run(GC<MODE>& c, unsigned ldsb, unsigned lane, unsigned wid, unsigned sx, unsigned sw, unsigned odd) {
  typedef GC<MODE> G;
  #pragma unroll
  for (int par = 0; par < 2; ++par) { c.xsrc[par] = G::LDX * (lane >> 3) + 16u * ((lane & 7u) ^ (4u * par + (lane >> 4))); c.wsrc[par] = MODE == 2 ? 16u * lane : wl_src(par, lane, G::LDW); }
  c.m0x = rfl(ldsb + 4096u * wid); c.m0w = rfl(ldsb + 16384u + 4096u * wid); c.m0e = rfl(ldsb + 32768u + 512u * wid);
  c.sx = rfl(sx + 32u * wid * G::LDX); c.sw = rfl(sw + (MODE == 2 ? 4096u * wid : 16u * wid * G::LDW)); c.swe = rfl(16384u - 3584u * wid);
  const unsigned wm = wid >> 1, wn = wid & 1u;
  #pragma unroll
  for (int ks = 0; ks < 2; ++ks) {
    const unsigned b = ldsb + 128u * (lane & 15u) + 16u * ((4u * ks + (lane >> 4)) ^ ((lane >> 1) & 7u));
    if constexpr (MODE == 2) { c.xa[0][ks][0] = b + 4096u * wid; c.xa[0][ks][1] = 0u; }
    else { c.xa[0][ks][0] = b + 8192u * wm + 4096u * wn; c.xa[0][ks][1] = b + 8192u * wm + 4096u * (wn ^ 1u); }
  }
  if constexpr (MODE == 2) { const unsigned b = ldsb + 16384u + w_rd<9>(lane); c.wr[0][0] = b + 256u * odd; c.wr[0][1] = c.wr[0][0] ^ 128u; c.wr[0][2] = b + (odd ? 0u : 2048u); c.wr[0][3] = c.wr[0][2] ^ 128u; }
  else {
    #pragma unroll
    for (int ju = 0; ju < 4; ++ju) c.wr[0][ju] = ldsb + 16384u + 2048u * (lane >> 4) + 1024u * wn + wl_rd((unsigned)ju ^ (2u * wm), lane);
  }
  #pragma unroll
  for (int mb = 0; mb < G::MB; ++mb)
    #pragma unroll
    for (int nb = 0; nb < G::NB; ++nb) { c.acc[mb][nb] = (f32x4){0.f, 0.f, 0.f, 0.f}; asm volatile("" : "+v"(c.acc[mb][nb])); }
  #pragma unroll
  for (int i = 0; i < 4; ++i) { c.wr[1][i] = c.wr[0][i] + 2u * G::STG; c.xa[1][i >> 1][i & 1] = c.xa[0][i >> 1][i & 1] + 2u * G::STG; asm volatile("" : "+v"(c.wr[1][i])); if (MODE != 2 || !(i & 1)) asm volatile("" : "+v"(c.xa[1][i >> 1][i & 1])); }
  c.cs[0] = c.cs[1] = c.rs[0] = c.rs[1] = c.rq[0] = c.rq[1] = (f32x4){0.f, 0.f, 0.f, 0.f};
  c.ones = __builtin_bit_cast(bf16x8, (u32x4){0x3f803f80u, 0x3f803f80u, 0x3f803f80u, 0x3f803f80u});
  if constexpr (MODE == 0) asm volatile("" : "+v"(c.cs[0]), "+v"(c.cs[1]), "+v"(c.ones), "+v"(c.rs[0]), "+v"(c.rs[1]), "+v"(c.rq[0]), "+v"(c.rq[1]));
  asm volatile("s_nop 4" ::: "memory");
  g_issue_x<MODE, 0>(c); g_issue_w<MODE, 0>(c); g_issue_x<MODE, 1>(c); g_issue_w<MODE, 1>(c); g_issue_x<MODE, 2>(c);
  if constexpr (MODE == 2) asm volatile("s_waitcnt vmcnt(13)\n\ts_barrier" ::: "memory"); else asm volatile("s_waitcnt vmcnt(12)\n\ts_barrier" ::: "memory");
  STAMP(MODE == 0 ? 0 : 2, 1);
  g_read_all<MODE, 0, 0>(c, make_iseq<G::MB + G::NB>{});
  GAP();
  if constexpr (NTK == 10) {
    g_tile<MODE, 0, 3>(c); g_tile<MODE, 1, 3>(c); g_tile<MODE, 2, 3>(c); g_tile<MODE, 3, 3>(c); g_tile<MODE, 0, 3>(c); g_tile<MODE, 1, 3>(c); g_tile<MODE, 2, 3>(c);
    g_tile<MODE, 3, 2>(c); g_tile<MODE, 0, 1>(c); g_tile<MODE, 1, 0>(c);
  } else {
    #pragma unroll 1
    for (int t = 0; t < NTK / 4 - 1; ++t) { g_tile<MODE, 0, 3>(c); g_tile<MODE, 1, 3>(c); g_tile<MODE, 2, 3>(c); g_tile<MODE, 3, 3>(c); }
    g_tile<MODE, 0, 3>(c); g_tile<MODE, 1, 2>(c); g_tile<MODE, 2, 1>(c); g_tile<MODE, 3, 0>(c);
  }
  asm volatile("s_nop 7\n\ts_nop 7" ::: "memory");
  GAP();
}

constexpr size_t SHM_K1 = 131072 + 2048;
extern "C" __global__ __launch_bounds__(256, 1)
void k1_ln_gemm1(const unsigned short* __restrict__ X, const unsigned short* __restrict__ WIN, unsigned short* __restrict__ XN,
                 unsigned short* __restrict__ Z, const unsigned short* __restrict__ WOUT, unsigned short* __restrict__ WOI, const unsigned short* __restrict__ WQ, unsigned* __restrict__ CNT) {
  extern __shared__ __attribute__((aligned(16))) char lds[];
  const unsigned tid = threadIdx.x, lane = tid & 63u, wid = rfl(tid >> 6);
  const unsigned bid = blockIdx.x, xcd = bid & 7u, jj = bid >> 3;
  if (jj >= 28u) {
    const unsigned t0 = ((jj - 28u) * 8u + xcd) * 256u + tid;
    if (t0 < 16u) CNT[32u * t0] = 0u;
    u32x4 v[9];
    #pragma unroll
    for (int i = 0; i < 9; ++i) { const unsigned id = t0 + 8192u * i; if (id < 69120u) v[i] = *(const u32x4*)((const char*)WOUT + 16u * id); }
    {
      __amdgpu_buffer_rsrc_t srdQ = __builtin_amdgcn_make_buffer_rsrc((void*)WQ, (short)0, DM * HD * 2, SRD_WORD3);
      const unsigned m0v = rfl((unsigned)(size_t)lds + 256u * wid);
      #pragma unroll
      for (int i = 0; i < 10; ++i) touch64(m0v, 64u * t0, srdQ, 524288u * i);
      unsigned long long pa, pb;
      asm volatile("s_getpc_b64 s[60:61]\n\ts_add_u32 s60, s60, k2_attn@rel32@lo+4\n\ts_addc_u32 s61, s61, k2_attn@rel32@hi+12\n\ts_mov_b64 %0, s[60:61]" : "=s"(pa) :: "scc", "s60", "s61");
      asm volatile("s_getpc_b64 s[60:61]\n\ts_add_u32 s60, s60, k3_dual@rel32@lo+4\n\ts_addc_u32 s61, s61, k3_dual@rel32@hi+12\n\ts_mov_b64 %0, s[60:61]" : "=s"(pb) :: "scc", "s60", "s61");
      const unsigned long long lo = pa < pb ? pa : pb, hi = pa < pb ? pb : pa;
      __amdgpu_buffer_rsrc_t srdC = __builtin_amdgcn_make_buffer_rsrc((void*)lo, (short)0, (int)(hi - lo) + 20480, SRD_WORD3);
      #pragma unroll
      for (int i = 0; i < 2; ++i) touch64(m0v, 64u * ((jj - 28u) * 256u + tid) + 65536u * i, srdC, 0u);
    }
    #pragma unroll
    for (int i = 0; i < 9; ++i) { const unsigned id = t0 + 8192u * i; if (id < 69120u) {
      const unsigned k = id / 80u, c16 = id - 80u * k, ct = c16 / 10u, cr = c16 - 10u * ct;
      const unsigned kb = 4u * (k >> 5) + ((k >> 2) & 3u), r = (k & 3u) + 4u * ((k >> 4) & 1u), slot = r ^ ((kb & 1u) << 2);
      *(u32x4*)((char*)WOI + ((ct * 108u + kb) * 5u + (cr >> 1)) * 256u + slot * 32u + (cr & 1u) * 16u) = v[i]; } }
    asm volatile("s_waitcnt vmcnt(0)" ::: "memory");
    return;
  }
  STAMP(0, 0);
  const unsigned rb = 4u * (xcd >> 1) + jj / 7u, ct = 7u * (xcd & 1u) + jj % 7u;
  const unsigned wm = wid >> 1, wn = wid & 1u;
  u32x4 xs[4];
  const size_t xoff = (size_t)(128u * rb + (tid >> 3)) * 1280u + 128u * (ct < 10u ? ct : 0u) + 16u * (tid & 7u);
  #pragma unroll
  for (int i = 0; i < 4; ++i) xs[i] = *(const u32x4*)((const char*)X + xoff + 32 * 1280 * i);
  GC<0> c;
  c.srdX = __builtin_amdgcn_make_buffer_rsrc((void*)X, (short)0, SEQ * DM * 2, SRD_WORD3);
  c.srdW = __builtin_amdgcn_make_buffer_rsrc((void*)WIN, (short)0, DM * FF * 2, SRD_WORD3);
  g_run<0, 10>(c, (unsigned)(size_t)lds, lane, wid, 128u * rb * 1280u, ct * 256u, 0u);
  STAMP(0, 4);
  f32x2* st = (f32x2*)(lds + 131072); float* cst = (float*)(lds + 131072 + 1024);
  #pragma unroll
  for (int i = 0; i < 2; ++i) {
    const unsigned e = lane & 3u;
    const float q = e == 0u ? c.rq[i][0] : e == 1u ? c.rq[i][1] : e == 2u ? c.rq[i][2] : c.rq[i][3];
    const float mean = c.rs[i][0] * (1.f / DM), var = fmaxf(q * (1.f / DM) - mean * mean, 0.f);
    if ((lane >> 4) == ((lane >> 2) & 3u)) st[64u * wm + 32u * wn + 16u * i + (lane & 15u)] = (f32x2){mean, 1.0f / sqrtf(var + 1e-5f)};
    if ((lane & 15u) == 0u) *(f32x4*)(cst + 64u * wn + 32u * wm + 16u * i + (lane >> 2)) = c.cs[i];
  }
  __syncthreads();
  f32x4 cs[4];
  #pragma unroll
  for (int nb = 0; nb < 4; ++nb) cs[nb] = *(const f32x4*)(cst + 64u * wn + 16u * ((unsigned)nb ^ (2u * wm)) + 4u * (lane >> 4));
  if (wn == 0u || ct < 13u) {
    #pragma unroll
    for (int mb = 0; mb < 4; ++mb) {
      const unsigned rb16 = 4u * wm + ((unsigned)mb ^ (2u * wn));
      const f32x2 mr = st[16u * rb16 + (lane & 15u)];
      #pragma unroll
      for (int pp = 0; pp < 2; ++pp) {
        u32x4 w;
        #pragma unroll
        for (int hh = 0; hh < 2; ++hh) {
          const f32x4 a = c.acc[mb][2 * pp + hh], s4 = cs[2 * pp + hh];
          w[2 * hh]     = gelu2(((f32x2){a[0], a[1]} - (f32x2){s4[0], s4[1]} * mr[0]) * mr[1]);
          w[2 * hh + 1] = gelu2(((f32x2){a[2], a[3]} - (f32x2){s4[2], s4[3]} * mr[0]) * mr[1]);
        }
        *(u32x4*)((char*)Z + ((size_t)(8u * rb + rb16) * 54u + 4u * ct + 2u * wn + ((unsigned)pp ^ wm)) * 1024u + 16u * lane) = w;
      }
    }
  }
  if (ct < 10u) {
    #pragma unroll
    for (int i = 0; i < 4; ++i) {
      const f32x2 mr = st[(tid >> 3) + 32u * i]; const float rc = mr[1] * QSCALE;
      u32x4 w;
      #pragma unroll
      for (int e = 0; e < 4; ++e) w[e] = cvtpk((bflo(xs[i][e]) - mr[0]) * rc, (bfhi(xs[i][e]) - mr[0]) * rc);
      *(u32x4*)((char*)XN + xoff + 32 * 1280 * i) = w;
    }
  }
  STAMP(0, 7);
}

DI void st16_dev(void* p, u32x4 v) { asm volatile("global_store_dwordx4 %0, %1, off sc1\n\ts_nop 1" :: "v"(p), "v"(v) : "memory"); }
DI void st8_dev(void* p, u32x2 v)  { asm volatile("global_store_dwordx2 %0, %1, off sc1\n\ts_nop 1" :: "v"(p), "v"(v) : "memory"); }
struct K4C { u32x4 uf[27]; bf16x8 W[4][5]; f32x4 acc[5]; unsigned wr0[3], wr1[3]; };
template <int J, int NBI> DI void k4_read(K4C& c) { c.W[J & 3][NBI] = w_frag(c.wr0[J / 9], c.wr1[J / 9], 5120u * (J % 9) + 256u * NBI); }
template <int J, int M> DI void k4_gap(K4C& c) {
  asm volatile("v_mfma_f32_16x16x32_bf16 %0, %1, %2, %0" : "+v"(c.acc[M]) : "v"(c.W[J & 3][M]), "v"(c.uf[J]));
  if constexpr (J + 3 < 27) k4_read<J + 3, M>(c);
  GAP();
}
#define UF9(o) "+v"(c.uf[o + 0]), "+v"(c.uf[o + 1]), "+v"(c.uf[o + 2]), "+v"(c.uf[o + 3]), "+v"(c.uf[o + 4]), "+v"(c.uf[o + 5]), "+v"(c.uf[o + 6]), "+v"(c.uf[o + 7]), "+v"(c.uf[o + 8])
template <int J> DI void k4_step(K4C& c) {
  if constexpr (J % 9 == 0) {
    if constexpr (J == 0)      asm volatile("s_waitcnt vmcnt(28)" : UF9(0) :: "memory");
    else if constexpr (J == 9) asm volatile("s_waitcnt vmcnt(19)" : UF9(9) :: "memory");
    else                       asm volatile("s_waitcnt vmcnt(10)" : UF9(18) :: "memory");
    STAMP(3, 2 + J / 9);
    GAP();
  }
  k4_gap<J, 0>(c); k4_gap<J, 1>(c); k4_gap<J, 2>(c); k4_gap<J, 3>(c); k4_gap<J, 4>(c);
}
template <int... Js> DI void k4_steps(K4C& c, iseq<Js...>) { (k4_step<Js>(c), ...); }
DI void k4_tail(const unsigned short* U, const unsigned short* __restrict__ WOI, const float* S, const unsigned short* __restrict__ X, unsigned short* __restrict__ OUT,
                unsigned* CNT, unsigned ldsb, unsigned tid, unsigned lane, unsigned wid, unsigned xcd, unsigned jj) {
  constexpr int NB = 5;
  const unsigned rb = 8u * (xcd >> 1) + 4u * (xcd & 1u) + (jj >> 3), ct = jj & 7u;
  const unsigned rb16 = 4u * rb + wid, row = 16u * rb16 + (lane & 15u);
  __amdgpu_buffer_rsrc_t srdW = __builtin_amdgcn_make_buffer_rsrc((void*)WOI, (short)0, FH * DM * 2, SRD_WORD3);
  STAMP(3, 0);
  const unsigned sw = rfl(ct * 138240u + 1024u * wid), m0b = rfl(ldsb + 1024u * wid), last = rfl(wid == 0u ? 45056u : 0u - 1024u * wid), vsrc = 16u * lane;
  asm volatile("s_nop 4" ::: "memory");
  #pragma unroll
  for (int G = 0; G < 3; ++G)
    #pragma unroll
    for (int p = 0; p < 12; ++p) dma16(m0b + 46080u * G + (p < 11 ? 4096u * p : last), vsrc, srdW, sw + 46080u * G + (p < 11 ? 4096u * p : last));
  u32x2 xv[NB];
  {
    const char* xp = (const char*)X + (size_t)row * 1280u + 2u * (ct * 80u + 4u * (lane >> 4));
    #pragma unroll
    for (int nb = 0; nb < NB; ++nb) xv[nb] = *(const u32x2*)(xp + 32 * nb);
  }
  if (tid == 0u) { while (__hip_atomic_load(CNT + 32u * (rb >> 1), __ATOMIC_RELAXED, __HIP_MEMORY_SCOPE_AGENT) < 16u) __builtin_amdgcn_s_sleep(2); }
  asm volatile("s_waitcnt vmcnt(0)\n\ts_barrier" ::: "memory");
  STAMP(3, 1);
  K4C c;
  f32x4 sv0[NB], sv1[NB];
  {
    const char* up = (const char*)U + (size_t)rb16 * (27u * 1024u) + 16u * lane;
    #pragma unroll
    for (int b = 0; b < 27; ++b) asm volatile("global_load_dwordx4 %0, %1, off offset:%2 sc1" : "=v"(c.uf[b]) : "v"(up + 4096 * (b >> 2)), "i"(1024 * (b & 3)) : "memory");
    const char* sp = (const char*)S + ((size_t)rb16 * 40u + 5u * ct) * 1024u + 16u * lane + 2048u;
    #pragma unroll
    for (int nb = 0; nb < NB; ++nb) {
      asm volatile("global_load_dwordx4 %0, %1, off offset:%2 sc1" : "=v"(sv0[nb]) : "v"(sp), "i"(1024 * nb - 2048) : "memory");
      asm volatile("global_load_dwordx4 %0, %1, off offset:%2 sc1" : "=v"(sv1[nb]) : "v"(sp + SEQ * DM * 4), "i"(1024 * nb - 2048) : "memory");
    }
  }
  #pragma unroll
  for (int nb = 0; nb < NB; ++nb) { c.acc[nb] = (f32x4){0.f, 0.f, 0.f, 0.f}; asm volatile("" : "+v"(c.acc[nb])); }
  #pragma unroll
  for (int G = 0; G < 3; ++G) { c.wr0[G] = ldsb + 46080u * G + w_rd<NB>(lane); c.wr1[G] = c.wr0[G] ^ 128u; asm volatile("" : "+v"(c.wr0[G]), "+v"(c.wr1[G])); }
  k4_read<0, 0>(c); k4_read<0, 1>(c); k4_read<0, 2>(c); k4_read<0, 3>(c); k4_read<0, 4>(c); k4_read<1, 0>(c); k4_read<1, 1>(c); k4_read<1, 2>(c); k4_read<1, 3>(c); k4_read<1, 4>(c);
  k4_read<2, 0>(c); k4_read<2, 1>(c); k4_read<2, 2>(c); k4_read<2, 3>(c); k4_read<2, 4>(c);
  GAP();
  k4_steps(c, make_iseq<27>{});
  asm volatile("s_nop 7\n\ts_nop 7" ::: "memory");
  GAP();
  asm volatile("s_waitcnt vmcnt(0)" : "+v"(sv0[0]), "+v"(sv0[1]), "+v"(sv0[2]), "+v"(sv0[3]), "+v"(sv0[4]), "+v"(sv1[0]), "+v"(sv1[1]), "+v"(sv1[2]), "+v"(sv1[3]), "+v"(sv1[4]),
               "+v"(xv[0]), "+v"(xv[1]), "+v"(xv[2]), "+v"(xv[3]), "+v"(xv[4]) :: "memory");
  STAMP(3, 5);
  char* op = (char*)OUT + (size_t)row * 1280u + 2u * (ct * 80u + 4u * (lane >> 4));
  #pragma unroll
  for (int nb = 0; nb < NB; ++nb) {
    float r[4];
    #pragma unroll
    for (int e = 0; e < 4; ++e) {
      const float yv = c.acc[nb][e], sg = yv * __builtin_amdgcn_rcpf(1.0f + __builtin_amdgcn_exp2f(-1.4426950408889634f * yv));
      const float xx = (e & 1) ? bfhi(xv[nb][e >> 1]) : bflo(xv[nb][e >> 1]);
      r[e] = xx + (sv0[nb][e] + sv1[nb][e]) * sg;
    }
    u32x2 w = {cvtpk(r[0], r[1]), cvtpk(r[2], r[3])};
    *(u32x2*)(op + 32 * nb) = w;
  }
  STAMP(3, 7);
}

constexpr size_t SHM_K3 = 4 * 34816;
extern "C" __global__ __launch_bounds__(256, 1)
void k3_dual(const unsigned short* __restrict__ O, const unsigned short* __restrict__ WO, float* S,
             const unsigned short* __restrict__ WSP, const unsigned short* __restrict__ Z2N, const unsigned short* __restrict__ Z,
             const unsigned short* __restrict__ BSP, unsigned short* U,
             const unsigned short* __restrict__ WOI, const unsigned short* __restrict__ X, unsigned short* __restrict__ OUT, unsigned* CNT) {
  extern __shared__ __attribute__((aligned(16))) char lds[];
  const unsigned tid = threadIdx.x, lane = tid & 63u, wid = rfl(tid >> 6);
  const unsigned bid = blockIdx.x, xcd = bid & 7u, jj = bid >> 3;
  const unsigned ldsb = (unsigned)(size_t)lds;
  STAMP(2, 0);
  const unsigned rb = 4u * (xcd >> 1) + (jj >= 12u ? (jj - 12u) / 5u : jj / 3u);
  if (jj >= 12u) {
    const unsigned j1 = jj - 12u, ct = j1 % 5u, kh = xcd & 1u;
    const unsigned wm = wid >> 1, wn = wid & 1u;
    GC<1> c;
    c.srdX = __builtin_amdgcn_make_buffer_rsrc((void*)O, (short)0, SEQ * HD * 2, SRD_WORD3);
    c.srdW = __builtin_amdgcn_make_buffer_rsrc((void*)WO, (short)0, HD * DM * 2, SRD_WORD3);
    g_run<1, 32>(c, ldsb, lane, wid, 128u * rb * (HD * 2) + kh * 4096u, kh * 2048u * (DM * 2) + ct * 256u, 0u);
    STAMP(2, 2);
    #pragma unroll
    for (int mb = 0; mb < 4; ++mb)
      #pragma unroll
      for (int nb = 0; nb < 4; ++nb)
        st16_dev((char*)S + ((size_t)(kh * 128u + 8u * rb + 4u * wm + ((unsigned)mb ^ (2u * wn))) * 40u + 8u * ct + 4u * wn + ((unsigned)nb ^ (2u * wm))) * 1024u + 16u * lane, __builtin_bit_cast(u32x4, c.acc[mb][nb]));
  } else {
    const unsigned ct = 3u * (xcd & 1u) + jj % 3u, odd = ct & 1u;
    const unsigned p0 = (9u * ct + odd) >> 1, bs1 = odd ? 9u * ct : 9u * ct + 8u;
    u32x4 z1v[2][4]; u32x2 z1s[2]; float bs[2];
    #pragma unroll
    for (int mb = 0; mb < 2; ++mb) {
      const unsigned rb16 = 8u * rb + 2u * wid + mb;
      bs[mb] = bflo(BSP[16u * rb16 + (lane & 15u)]);
      #pragma unroll
      for (int pp = 0; pp < 4; ++pp) z1v[mb][pp] = *(const u32x4*)((const char*)Z + ((size_t)rb16 * 54u + p0 + pp) * 1024u + 16u * lane);
      z1s[mb] = *(const u32x2*)((const char*)Z + ((size_t)rb16 * 54u + (bs1 >> 1)) * 1024u + 16u * lane + 8u * (bs1 & 1u));
    }
    GC<2> c;
    c.srdX = __builtin_amdgcn_make_buffer_rsrc((void*)WSP, (short)0, SEQ * SEQ * 2, SRD_WORD3);
    c.srdW = __builtin_amdgcn_make_buffer_rsrc((void*)Z2N, (short)0, SEQ * FH * 2, SRD_WORD3);
    g_run<2, 32>(c, ldsb, lane, wid, 128u * rb * (SEQ * 2), ct * (32u * 18432u), odd);
    STAMP(2, 2);
    #pragma unroll
    for (int mb = 0; mb < 2; ++mb) {
      asm volatile("" : "+v"(z1v[mb][0]), "+v"(z1v[mb][1]), "+v"(z1v[mb][2]), "+v"(z1v[mb][3]), "+v"(z1s[mb]), "+v"(bs[mb]) :: "memory");
      const unsigned rb16 = 8u * rb + 2u * wid + mb; const float b = bs[mb];
      #pragma unroll
      for (int pp = 0; pp < 4; ++pp) {
        const f32x4 g0 = c.acc[mb][2 * pp], g1 = c.acc[mb][2 * pp + 1]; const u32x4 z = z1v[mb][pp];
        u32x4 w = {cvtpk(bflo(z[0]) * (g0[0] + b), bfhi(z[0]) * (g0[1] + b)), cvtpk(bflo(z[1]) * (g0[2] + b), bfhi(z[1]) * (g0[3] + b)),
                   cvtpk(bflo(z[2]) * (g1[0] + b), bfhi(z[2]) * (g1[1] + b)), cvtpk(bflo(z[3]) * (g1[2] + b), bfhi(z[3]) * (g1[3] + b))};
        st16_dev((char*)U + ((size_t)rb16 * 27u + p0 + pp) * 1024u + 16u * lane, w);
      }
      const f32x4 g = c.acc[mb][8]; const u32x2 z = z1s[mb];
      u32x2 w = {cvtpk(bflo(z[0]) * (g[0] + b), bfhi(z[0]) * (g[1] + b)), cvtpk(bflo(z[1]) * (g[2] + b), bfhi(z[1]) * (g[3] + b))};
      st8_dev((char*)U + ((size_t)rb16 * 27u + (bs1 >> 1)) * 1024u + 16u * lane + 8u * (bs1 & 1u), w);
    }
  }
  asm volatile("s_waitcnt vmcnt(0)\n\ts_barrier" ::: "memory");
  if (tid == 0u) __hip_atomic_fetch_add(CNT + 32u * rb, 1u, __ATOMIC_RELAXED, __HIP_MEMORY_SCOPE_AGENT);
  STAMP(2, 7);
  k4_tail(U, WOI, S, X, OUT, CNT, ldsb, tid, lane, wid, xcd, jj);
}


constexpr size_t WS_XN = 0, WS_Z1 = WS_XN + (size_t)SEQ * DM * 2, WS_Z2 = WS_Z1 + (size_t)SEQ * FH * 2, WS_Z2N = WS_Z2 + (size_t)SEQ * FH * 2,
                 WS_U = WS_Z2N + (size_t)SEQ * FH * 2, WS_O = WS_U + (size_t)SEQ * FH * 2, WS_S = WS_O + (size_t)SEQ * HD * 2, WS_WOI = WS_S + (size_t)2 * SEQ * DM * 4, WS_CNT = WS_WOI + (size_t)FH * DM * 2, WS_END = WS_CNT + 2048;
extern "C" void kernel_launch(void* const* d_in, const int* in_sizes, int n_in, void* d_out, int out_size, void* d_ws, size_t ws_size, hipStream_t stream) {
  typedef const unsigned short* cus;
  typedef unsigned short* us;
  cus x = (cus)d_in[0], kc = (cus)d_in[1], vc = (cus)d_in[2], wq = (cus)d_in[3], wo = (cus)d_in[4];
  cus win = (cus)d_in[6], wsp = (cus)d_in[7], bsp = (cus)d_in[8], wout = (cus)d_in[9];
  char* ws = (char*)d_ws;
  us xn = (us)(ws + WS_XN), z1 = (us)(ws + WS_Z1), woi = (us)(ws + WS_WOI), z2n = (us)(ws + WS_Z2N), u = (us)(ws + WS_U), o = (us)(ws + WS_O);
  float* s = (float*)(ws + WS_S); unsigned* cnt = (unsigned*)(ws + WS_CNT);
  (void)hipFuncSetAttribute((const void*)k1_ln_gemm1, hipFuncAttributeMaxDynamicSharedMemorySize, (int)SHM_K1);
  (void)hipFuncSetAttribute((const void*)k2_attn, hipFuncAttributeMaxDynamicSharedMemorySize, (int)SHM_ATTN);
  (void)hipFuncSetAttribute((const void*)k3_dual, hipFuncAttributeMaxDynamicSharedMemorySize, (int)SHM_K3);
  hipLaunchKernelGGL(k1_ln_gemm1, dim3(256), dim3(256), SHM_K1, stream, x, win, xn, z1, wout, woi, wq, cnt);
  hipLaunchKernelGGL(k2_attn, dim3(256), dim3(256), SHM_ATTN, stream, (cus)xn, wq, kc, vc, (cus)z1, z2n, o, wsp, wo);
  hipLaunchKernelGGL(k3_dual, dim3(256), dim3(256), SHM_K3, stream, (cus)o, wo, s, wsp, (cus)z2n, (cus)z1, bsp, u, (cus)woi, x, (us)d_out, cnt);
}
```
